```python
import torch

def _softmax(x, axis=-1):
    return torch.softmax(x, dim=axis)
def _gelu(x):
    return torch.nn.functional.gelu(x, approximate="tanh")
def _silu(x):
    return torch.nn.functional.silu(x)
def _sigmoid(x):
    return torch.sigmoid(x)
def _layernorm(x, axis=-1, eps=1e-5):
    m = x.mean(dim=axis, keepdim=True)
    v = x.var(dim=axis, keepdim=True, unbiased=False)
    return (x - m) / torch.sqrt(v + eps)
def _scatter_add(tgt, idx, src, axis=0):
    return tgt.clone().index_add_(axis, idx, src)
def _segment_sum(src, seg, n):
    out = torch.zeros((n,) + src.shape[1:], dtype=src.dtype)
    return out.index_add_(0, seg, src)
def _pad(x, pw):
    flat = tuple(v for pair in reversed(pw) for v in pair)
    return torch.nn.functional.pad(x, flat)

kernel_name = 'parallel_sum__ssm_conv_s4__dense_activation_gelu__prompt'

def setup_inputs():

    _w = (lambda a: (lambda t: t.to(torch.bfloat16) if t.is_floating_point() else t)(torch.as_tensor(a)))
    def _A_setup():
        B, L, D, N = 1, 2048, 576, 128
        A_re = -(lambda _x: torch.exp(torch.as_tensor(_x)))((lambda *s: torch.randn(*s, dtype=torch.bfloat16) * 6.4)(D, N))
        A_im = torch.pi * (lambda *s: torch.randn(*s, dtype=torch.bfloat16) * 0.05)(D, N)
        B_re = (lambda *s: torch.randn(*s, dtype=torch.bfloat16) * 0.1)(D, N)
        B_im = (lambda *s: torch.randn(*s, dtype=torch.bfloat16) * 0.1)(D, N)
        C_re = (lambda *s: torch.randn(*s, dtype=torch.bfloat16) * 0.1)(D, N)
        C_im = (lambda *s: torch.randn(*s, dtype=torch.bfloat16) * 0.1)(D, N)
        log_dt = (lambda *s: torch.randn(*s, dtype=torch.bfloat16) * 0.05)(D) * 0.5 - 4.0
        Dskip = (lambda *s: torch.randn(*s, dtype=torch.bfloat16) * 0.05)(D)
        return {
            "u": _w((lambda *s: torch.randn(*s, dtype=torch.bfloat16) * 0.05)(B, L, D)),
            "A_re": _w(A_re),
            "A_im": _w(A_im),
            "B_re": _w(B_re),
            "B_im": _w(B_im),
            "C_re": _w(C_re),
            "C_im": _w(C_im),
            "log_dt": _w(log_dt),
            "Dskip": _w(Dskip),
        }
    _A_in = _A_setup()

    def _B_setup():
        B, S, D, F = 1, 2048, 576, 4608
        return {
            "x": _w((lambda *s: torch.randn(*s, dtype=torch.bfloat16) * 0.05)(B, S, D)),
            "w1": _w((lambda *s: torch.randn(*s, dtype=torch.bfloat16) * 0.8)(D, F) * (D ** -0.5)),
            "b1": _w((lambda *s: torch.randn(*s, dtype=torch.bfloat16) * 0.2)(F) * 0.01),
            "w2": _w((lambda *s: torch.randn(*s, dtype=torch.bfloat16) * 0.2)(F, D) * (F ** -0.5)),
            "b2": _w((lambda *s: torch.randn(*s, dtype=torch.bfloat16) * 0.2)(D) * 0.01),
        }
    _B_in = _B_setup()

    return {
        "A_u": _A_in["u"],
        "A_A_re": _A_in["A_re"],
        "A_A_im": _A_in["A_im"],
        "A_B_re": _A_in["B_re"],
        "A_B_im": _A_in["B_im"],
        "A_C_re": _A_in["C_re"],
        "A_C_im": _A_in["C_im"],
        "A_log_dt": _A_in["log_dt"],
        "A_Dskip": _A_in["Dskip"],
        "B_x": _B_in["x"],
        "B_w1": _B_in["w1"],
        "B_b1": _B_in["b1"],
        "B_w2": _B_in["w2"],
        "B_b2": _B_in["b2"],
    }

def reference(A_u, A_A_re, A_A_im, A_B_re, A_B_im, A_C_re, A_C_im, A_log_dt, A_Dskip, B_x, B_w1, B_b1, B_w2, B_b2):
    def _A_ref(u, A_re, A_im, B_re, B_im, C_re, C_im, log_dt, Dskip):
        Bsz, L, D = u.shape
        dt = torch.exp(log_dt)[:, None]
        A = A_re + 1j * A_im
        Bc = B_re + 1j * B_im
        Cc = C_re + 1j * C_im
        logdA = dt * A
        dB = dt * Bc
        l = (lambda x, dt: x.to(dt))(torch.arange(L), A_re.dtype)
        P = torch.exp(logdA[:, :, None] * l[None, None, :])
        K = torch.einsum("dn,dnl,dn->dl", Cc, P, dB).real
        L2 = 2 * L
        Kp = _pad(K, ((0, 0), (0, L)))
        up = (lambda x, *d: x.permute(*d))(u, 0, 2, 1)
        up = _pad(up, ((0, 0), (0, 0), (0, L)))
        Kf = (lambda x, axes=(-2,-1): torch.fft.rfft2(x.float(), dim=axes))(Kp[:, None, :], axes=(-2, -1))
        Uf = (lambda x, axes=(-2,-1): torch.fft.rfft2(x.float(), dim=axes))(up[:, :, None, :], axes=(-2, -1))
        Yf = Uf * Kf[None, :, :, :]
        y = (lambda x, s, axes=(-2,-1): torch.fft.irfft2(x, s=s, dim=axes).to(torch.bfloat16))(Yf, (1, L2), axes=(-2, -1))[:, :, 0, :L]
        y = (lambda x, *d: x.permute(*d))(y, 0, 2, 1) + u * Dskip[None, None, :]
        return y

    def _B_ref(x, w1, b1, w2, b2):
        h = torch.einsum("bsd,df->bsf", x, w1) + b1
        h = _gelu(h)
        return torch.einsum("bsf,fd->bsd", h, w2) + b2

    x = A_u
    xn = _layernorm(x, axis=-1)
    return x + _A_ref(xn, A_A_re, A_A_im, A_B_re, A_B_im, A_C_re, A_C_im, A_log_dt, A_Dskip) + _B_ref(xn, B_w1, B_b1, B_w2, B_b2)
```

```cpp
#include <hip/hip_runtime.h>
#include <hip/hip_bf16.h>
#include <stdint.h>

typedef unsigned short u16;
constexpr int L = 2048, D = 576, N = 128, F = 4608;

__device__ __forceinline__ float bf2f(u16 v) { return __uint_as_float(((unsigned)v) << 16); }
__device__ __forceinline__ u16 f2bf(float f) {
    __hip_bfloat16 b = __float2bfloat16(f);
    return *reinterpret_cast<u16*>(&b);
}
__device__ __forceinline__ float rbf(float f) { return bf2f(f2bf(f)); }

__global__ void ln_kernel(const u16* x, u16* xn) {
    int row = blockIdx.x, lane = threadIdx.x;
    float v[9], s = 0.f;
    for (int i = 0; i < 9; ++i) { v[i] = bf2f(x[row * D + lane + 64 * i]); s += v[i]; }
    for (int o = 32; o; o >>= 1) s += __shfl_xor(s, o);
    float m = s / D, q = 0.f;
    for (int i = 0; i < 9; ++i) q += (v[i] - m) * (v[i] - m);
    for (int o = 32; o; o >>= 1) q += __shfl_xor(q, o);
    float var = rbf(q / D), mb = rbf(m);
    float sd = rbf(sqrtf(rbf(var + 1e-5f)));
    for (int i = 0; i < 9; ++i) xn[row * D + lane + 64 * i] = f2bf(rbf(v[i] - mb) / sd);
}

__global__ void kgen_kernel(const u16* Are, const u16* Aim, const u16* Bre, const u16* Bim,
                            const u16* Cre, const u16* Cim, const u16* logdt, float* K) {
    int d = blockIdx.y, l = blockIdx.x * 256 + threadIdx.x;
    float dt = rbf(expf(bf2f(logdt[d])));
    float lf = rbf((float)l), acc = 0.f;
    for (int n = 0; n < N; ++n) {
        int i = d * N + n;
        float a = dt * bf2f(Are[i]), b = dt * bf2f(Aim[i]);
        float br = dt * bf2f(Bre[i]), bi = dt * bf2f(Bim[i]);
        float cr = bf2f(Cre[i]), ci = bf2f(Cim[i]);
        float wr = cr * br - ci * bi, wi = cr * bi + ci * br;
        float e = expf(a * lf), sn, cs;
        sincosf(b * lf, &sn, &cs);
        acc += e * (wr * cs - wi * sn);
    }
    K[d * L + l] = acc;
}

__global__ void conv_kernel(const u16* xn, const float* K, float* y) {
    int d = blockIdx.x * 64 + threadIdx.x, t = blockIdx.y;
    float acc = 0.f;
    for (int l = 0; l <= t; ++l) acc += K[d * L + l] * bf2f(xn[(t - l) * D + d]);
    y[t * D + d] = acc;
}

__global__ void gemm1_kernel(const u16* xn, const u16* w1, const u16* b1, u16* h) {
    int f = blockIdx.x * 256 + threadIdx.x, t = blockIdx.y;
    float acc = 0.f;
    for (int k = 0; k < D; ++k) acc += bf2f(xn[t * D + k]) * bf2f(w1[k * F + f]);
    float v = rbf(rbf(acc) + bf2f(b1[f]));
    float u = 0.7978845608028654f * (v + 0.044715f * v * v * v);
    h[(size_t)t * F + f] = f2bf(0.5f * v * (1.f + tanhf(u)));
}

__global__ void gemm2_kernel(const u16* h, const u16* w2, const u16* b2, const u16* x, const u16* xn,
                             const u16* dskip, const float* y, u16* out) {
    int d = blockIdx.x * 64 + threadIdx.x, t = blockIdx.y;
    float acc = 0.f;
    for (int k = 0; k < F; ++k) acc += bf2f(h[(size_t)t * F + k]) * bf2f(w2[k * D + d]);
    float bb = rbf(rbf(acc) + bf2f(b2[d]));
    float aa = rbf(rbf(y[t * D + d]) + rbf(bf2f(xn[t * D + d]) * bf2f(dskip[d])));
    out[t * D + d] = f2bf(rbf(bf2f(x[t * D + d]) + aa) + bb);
}

extern "C" void kernel_launch(void* const* d_in, const int* in_sizes, int n_in,
                              void* d_out, int out_size, void* d_ws, size_t ws_size,
                              hipStream_t stream) {
    const u16* x = (const u16*)d_in[0];
    char* ws = (char*)d_ws;
    u16* xn = (u16*)ws;
    float* K = (float*)(ws + (4 << 20));
    float* y = (float*)(ws + (12 << 20));
    u16* h = (u16*)(ws + (20 << 20));
    ln_kernel<<<L, 64, 0, stream>>>(x, xn);
    kgen_kernel<<<dim3(L / 256, D), 256, 0, stream>>>((const u16*)d_in[1], (const u16*)d_in[2], (const u16*)d_in[3],
        (const u16*)d_in[4], (const u16*)d_in[5], (const u16*)d_in[6], (const u16*)d_in[7], K);
    conv_kernel<<<dim3(D / 64, L), 64, 0, stream>>>(xn, K, y);
    gemm1_kernel<<<dim3(F / 256, L), 256, 0, stream>>>(xn, (const u16*)d_in[10], (const u16*)d_in[11], h);
    gemm2_kernel<<<dim3(D / 64, L), 64, 0, stream>>>(h, (const u16*)d_in[12], (const u16*)d_in[13], x, xn,
        (const u16*)d_in[8], y, (u16*)d_out);
}
```

```cpp
#include <hip/hip_runtime.h>
#include <stdint.h>

typedef unsigned short u16;
typedef unsigned int u32;
typedef __attribute__((ext_vector_type(2))) __bf16 bf16x2;
typedef __attribute__((ext_vector_type(8))) __bf16 bf16x8;
typedef __attribute__((ext_vector_type(4))) short s16x4;
typedef __attribute__((ext_vector_type(2))) float f32x2;
typedef __attribute__((ext_vector_type(4))) float f32x4;
typedef __attribute__((ext_vector_type(2))) unsigned u32x2;
typedef __attribute__((ext_vector_type(4))) unsigned u32x4;
#define LAS __attribute__((address_space(3)))
#define GAS __attribute__((address_space(1)))
#define CAS __attribute__((address_space(4)))
#define DI __device__ __forceinline__
#define AI __attribute__((always_inline))
#if defined(__HIP_DEVICE_COMPILE__)
#pragma clang attribute push(__attribute__((target("no-packed-fp32-ops"))), apply_to = function)
#endif
#define SB() __builtin_amdgcn_sched_barrier(0)
#define CFENCE() asm volatile("" ::: "memory")
#define SYNC() do { __builtin_amdgcn_fence(__ATOMIC_RELEASE, "workgroup"); __builtin_amdgcn_s_barrier(); __builtin_amdgcn_fence(__ATOMIC_ACQUIRE, "workgroup"); } while (0)
#define MFMA_SETTLE() do { SB(); asm volatile("s_nop 15\n\ts_nop 15"); SB(); } while (0)
#define WAIT_VM(n) asm volatile("s_waitcnt vmcnt(%0)" :: "i"(n) : "memory")
#define WAIT_LGKM0() do { CFENCE(); __builtin_amdgcn_s_waitcnt(0xC07F); CFENCE(); } while (0)
#define RLX_AGENT __ATOMIC_RELAXED, __HIP_MEMORY_SCOPE_AGENT
#define RLX_WG __ATOMIC_RELAXED, __HIP_MEMORY_SCOPE_WORKGROUP

constexpr int SEQ = 2048, DM = 576, NST = 128;
constexpr int KC_STRIDE = 1344;
constexpr int FLAG_STRIDE = 128;
constexpr size_t WS_FLAG = 0;
constexpr size_t WS_XNA = 768 * FLAG_STRIDE;
constexpr size_t WS_UT = WS_XNA + (size_t)SEQ * DM * 2;
constexpr size_t WS_YB = WS_UT + (size_t)SEQ * DM * 2;
constexpr size_t WS_KC = WS_YB + (size_t)SEQ * DM * 2;
constexpr size_t WS_PART = WS_KC + (size_t)DM * KC_STRIDE;

DI float bf_lo(u32 v) { return __builtin_bit_cast(float, v << 16); }
DI float bf_hi(u32 v) { return __builtin_bit_cast(float, v & 0xffff0000u); }
DI float bf2f(u16 v) { return __builtin_bit_cast(float, (u32)v << 16); }
DI u32 pack2(float lo, float hi) {
    f32x2 v = {lo, hi};
    return __builtin_bit_cast(u32, __builtin_convertvector(v, bf16x2));
}
DI float rbf(float f) { return bf_lo(pack2(f, f)); }
DI f32x4 mfma16(u32x4 a, u32x4 b, f32x4 c) {
    return __builtin_amdgcn_mfma_f32_16x16x32_bf16(__builtin_bit_cast(bf16x8, a), __builtin_bit_cast(bf16x8, b), c, 0, 0, 0);
}
DI u32x2 tr_read(u32 addr) {
    return __builtin_bit_cast(u32x2, __builtin_amdgcn_ds_read_tr16_b64_v4i16((LAS s16x4*)addr));
}
DI u32x4 lds_r128(u32 addr) { return *(LAS u32x4*)addr; }
DI void lds_w128(u32 addr, u32x4 v) { *(LAS u32x4*)addr = v; }
DI void lds_w64(u32 addr, u32x2 v) { *(LAS u32x2*)addr = v; }
DI void lds_w32(u32 addr, u32 v) { *(LAS u32*)addr = v; }
DI float xsum(float v, int lane) {
    v += __builtin_bit_cast(float, __builtin_amdgcn_ds_bpermute((lane ^ 16) << 2, __builtin_bit_cast(int, v)));
    v += __builtin_bit_cast(float, __builtin_amdgcn_ds_bpermute((lane ^ 32) << 2, __builtin_bit_cast(int, v)));
    return v;
}

struct cpx { float re, im; };
DI cpx cmul(cpx a, cpx b) { return cpx{a.re * b.re - a.im * b.im, a.re * b.im + a.im * b.re}; }

constexpr int KG_ROW = 528, KG_CH = 57 * KG_ROW + 48;
struct PrepArgs { const u16 *x, *Are, *Aim, *Bre, *Bim, *Cre, *Cim, *logdt; char* ws; };

__global__ __launch_bounds__(576) void prep_kernel(PrepArgs p) {
    __shared__ __attribute__((aligned(16))) char smem[2 * KG_CH];
    const int tid = __builtin_amdgcn_workitem_id_x(), bid = __builtin_amdgcn_workgroup_id_x();
    const int lane = tid & 63, w = __builtin_amdgcn_readfirstlane(tid >> 6), r16 = lane & 15, q = lane >> 4;
    const u32 sb = (u32)(size_t)(LAS char*)smem;
    char* const ws = p.ws;
    if (bid < 128) {
        const int tt = bid;
        float* red = (float*)(smem + 21504);
        const u16* xr = p.x + (size_t)(tt * 16 + r16) * DM + w * 64 + q * 8;
        const u32x4 v0 = *(const u32x4*)xr, v1 = *(const u32x4*)(xr + 32);
        if (tid < 6) *(u32*)(ws + WS_FLAG + (tt * 6 + tid) * FLAG_STRIDE) = 0u;
        float f[16];
#pragma unroll
        for (int i = 0; i < 4; ++i) { f[2*i] = bf_lo(v0[i]); f[2*i+1] = bf_hi(v0[i]); f[8+2*i] = bf_lo(v1[i]); f[9+2*i] = bf_hi(v1[i]); }
        float s = 0.f;
#pragma unroll
        for (int i = 0; i < 16; ++i) s += f[i];
        s = xsum(s, lane);
        const float lm = s * (1.f / 64);
        float ss = 0.f;
#pragma unroll
        for (int i = 0; i < 16; ++i) ss += (f[i] - lm) * (f[i] - lm);
        ss = xsum(ss, lane);
        if (q == 0) { red[w * 16 + r16] = lm; red[144 + w * 16 + r16] = ss; }
        SYNC();
        float m = 0.f, var = 0.f, lms[9];
#pragma unroll
        for (int i = 0; i < 9; ++i) { lms[i] = red[i * 16 + r16]; m += lms[i]; var += red[144 + i * 16 + r16]; }
        m *= (1.f / 9);
#pragma unroll
        for (int i = 0; i < 9; ++i) var += 64.f * (lms[i] - m) * (lms[i] - m);
        var = rbf(var * (1.f / DM));
        const float mb = rbf(m), rs = 1.f / rbf(__builtin_sqrtf(rbf(var + 1e-5f)));
        u32x4 o0, o1;
#pragma unroll
        for (int i = 0; i < 4; ++i) {
            const u32 a = pack2(f[2*i] - mb, f[2*i+1] - mb), b = pack2(f[8+2*i] - mb, f[9+2*i] - mb);
            o0[i] = pack2(bf_lo(a) * rs, bf_hi(a) * rs);
            o1[i] = pack2(bf_lo(b) * rs, bf_hi(b) * rs);
        }
        u32x4* xa = (u32x4*)(ws + WS_XNA) + (size_t)(tt * 18 + w * 2) * 64 + lane;
        xa[0] = o0; xa[64] = o1;
        const u32 img = sb + w * 2304;
        lds_w128(img + r16 * 144 + q * 16, o0);
        lds_w128(img + r16 * 144 + 64 + q * 16, o1);
        char* ut = ws + WS_UT + ((size_t)tt * DM + w * 64) * 32 + r16 * 32 + q * 8;
#pragma unroll
        for (int c = 0; c < 4; ++c) {
            const u32x2 t = tr_read(img + (4 * q + (r16 >> 2)) * 144 + c * 32 + (r16 & 3) * 8);
            *(u32x2*)(ut + c * 512) = t;
        }
    } else {
        if (w == 8) return;
        const int d = (bid - 128) * 2 + (w >> 2), n = (w & 1) * 64 + lane, part = (w >> 1) & 1;
        const u32 img = sb + (w >> 2) * KG_CH;
        {
            const int o = d * NST + n;
            const float are = bf2f(p.Are[o]), aim = bf2f(p.Aim[o]), bre = bf2f(p.Bre[o]), bim = bf2f(p.Bim[o]);
            const float cre = bf2f(p.Cre[o]), cim = bf2f(p.Cim[o]);
            const float dt = rbf(__builtin_expf(bf2f(p.logdt[d])));
            const float a2 = dt * are * 1.4426950408889634f, bt = dt * aim * 0.15915494309189535f;
            const cpx c = cmul(cpx{cre, cim}, cpx{dt * bre, dt * bim});
            auto cexp = [&](float l) AI {
                const float e = __builtin_amdgcn_exp2f(a2 * l), ph = __builtin_amdgcn_fractf(bt * l);
                return cpx{e * __builtin_amdgcn_cosf(ph), e * __builtin_amdgcn_sinf(ph)};
            };
            const u32 wa = img + n * 4;
            auto putA = [&](int row, cpx v) AI { lds_w32(wa + row * KG_ROW, pack2(v.re, -v.im)); };
            auto putB = [&](int row, cpx v) AI { lds_w32(wa + (41 + row) * KG_ROW, pack2(v.re, v.im)); };
            const cpx e128 = cexp(128.f);
            if (part == 0) {
                const cpx e1 = cexp(1.f), e256 = cexp(256.f);
                cpx r = c;
#pragma unroll
                for (int i = 0; i < 8; ++i) {
                    const cpx r2 = cmul(r, e128);
                    putA(i, r); putA(8 + i, r2);
                    if ((i & 1) == 0) {
                        const cpx g = cmul(r, e256), g2 = cmul(r2, e256);
                        putA(16 + (i >> 1), g); putA(20 + (i >> 1), g2);
                        if ((i & 3) == 0) {
                            const cpx h = cmul(g, e256), h2 = cmul(g2, e256);
                            putA(24 + (i >> 2), h); putA(26 + (i >> 2), h2);
                            putA(28 + (i >> 2), cmul(h, e256)); putA(30 + (i >> 2), cmul(h2, e256));
                        }
                    }
                    if (i < 7) r = cmul(r, e1);
                }
            } else {
                const cpx e8 = cexp(8.f), e64 = cexp(64.f);
                cpx r = cmul(c, cexp(1024.f)), b = cpx{1.f, 0.f};
#pragma unroll
                for (int i = 0; i < 9; ++i) {
                    putA(32 + i, r);
                    if (i < 8) {
                        r = cmul(r, e128);
                        putB(i, b); putB(8 + i, i == 0 ? e64 : cmul(b, e64));
                        b = i == 0 ? e8 : cmul(b, e8);
                    }
                }
            }
        }
        SYNC();
        const int tl = w & 3;
        if (tl < 3) {
            u32x4 fa[8], fb[8];
#pragma unroll
            for (int k = 0; k < 8; ++k) {
                fa[k] = lds_r128(img + (tl * 16 + r16) * KG_ROW + k * 64 + q * 16);
                fb[k] = lds_r128(img + (41 + r16) * KG_ROW + k * 64 + q * 16);
            }
            f32x4 acc = {0.f, 0.f, 0.f, 0.f};
            if (tl < 2) {
#pragma unroll
                for (int k = 0; k < 8; ++k) acc = mfma16(fa[k], fb[k], acc);
            } else {
#pragma unroll
                for (int k = 0; k < 8; ++k) acc = mfma16(fb[k], fa[k], acc);
            }
            MFMA_SETTLE();
            const u32x2 o = {pack2(acc[0], acc[1]), pack2(acc[2], acc[3])};
            char* kc = ws + WS_KC + (size_t)d * KC_STRIDE;
            if (tl == 0) *(u32x2*)(kc + 2 * (128 * (q >> 1) + 4 * (q & 1) + 8 * r16)) = o;
            else if (tl == 2) { if (r16 <= 8) *(u32x2*)(kc + 2 * (512 + 16 * r16 + 4 * q)) = o; }
            else if (q < 2) *(u32x2*)(kc + 2 * (256 + 64 * q + 4 * r16)) = o;
            else {
                *(u32*)(kc + 2 * (384 + 64 * (q - 2) + 2 * r16)) = o[0];
                *(u32*)(kc + 2 * (416 + 64 * (q - 2) + 2 * r16)) = o[1];
            }
        }
    }
}

DI void glds16(const char* sbase, u32 voff, u32 lds) {
    asm volatile("s_mov_b32 m0, %2\n\ts_nop 0\n\tglobal_load_lds_dwordx4 %0, %1" :: "v"(voff), "s"(sbase), "s"(lds) : "memory");
}
template <int OFF>
DI void glds16_next(const char* sbase, u32 voff) {
    asm volatile("global_load_lds_dwordx4 %0, %1 offset:%2" :: "v"(voff), "s"(sbase), "i"(OFF) : "memory");
}
DI float gelu(float v) {
    const float c0 = -2.f * 0.7978845608028654f * 1.4426950408889634f, c1 = c0 * 0.044715f;
    const float e = __builtin_amdgcn_exp2f(v * __builtin_fmaf(v * v, c1, c0));
    return v * __builtin_amdgcn_rcpf(1.f + e);
}
#define STS(i)

constexpr int A_BYTES = 73728;
constexpr int L_RINGA = A_BYTES, L_RINGB = L_RINGA + 40960;
constexpr int KR_ODD = 4160, CV_LDS = 4608 + 8320;
constexpr int L_CONV = L_RINGB, L_KC = L_CONV + 3 * CV_LDS, L_BIAS = 157696, L_CNT = 159744;
constexpr int MAIN_LDS = 163840;
constexpr int SPIN_CAP = 1 << 17;
enum { C_X = 0, C_KE, C_CV, C_1A, C_1B, C_HA, C_HB, C_Y, C_PA, C_PB };

DI u32 arrive(u32 sb, int c, int lane) {
    u32 r = 0;
    CFENCE();
    if (lane == 0) r = __hip_atomic_fetch_add((LAS u32*)(sb + L_CNT + c * 4), 1u, RLX_WG);
    CFENCE();
    return r;
}
DI void await(u32 sb, int c, u32 n) {
    CFENCE();
#pragma unroll 1
    for (int i = 0; i < SPIN_CAP; ++i) {
        if ((u32)__builtin_amdgcn_readfirstlane(__hip_atomic_load((LAS u32*)(sb + L_CNT + c * 4), RLX_WG)) >= n) break;
        __builtin_amdgcn_s_sleep(1);
    }
    CFENCE();
}

constexpr int hi_bit(int m) { int r = 0; for (int i = 0; i < 8; ++i) if (m >> i & 1) r = i; return r; }
constexpr int pop_below(int m, int b) { int r = 0; for (int i = 0; i < b; ++i) r += m >> i & 1; return r; }
constexpr int n_mfma(int m) { int r = 0; for (int i = 0; i < 8; ++i) if (m >> i & 1) r += i + 1; return r; }

template <int MASK>
DI void conv_part(u32 ub, float ds, int lane, u32x2* yo) {
    constexpr int H = hi_bit(MASK) + 1, NA = pop_below(MASK, 8), NM = n_mfma(MASK);
    const int r16 = lane & 15, q = lane >> 4;
    u32 ka = ub + 4608 + ((r16 & 1) ? (2031 + 8 * q - r16) * 2 : KR_ODD + (2030 + 8 * q - r16) * 2) - 512 * (H - 1);
    u32 ua = ub + 448 + r16 * 32 + q * 16;
    f32x4 acc[NA];
#pragma unroll
    for (int i = 0; i < NA; ++i) acc[i] = f32x4{0.f, 0.f, 0.f, 0.f};
    u32x4 fa[2][H], fb[2][H];
    auto ld = [&](u32x4* pa, u32x4* pb) AI {
#pragma unroll
        for (int a = 0; a < H; ++a) {
            const LAS u32* p = (const LAS u32*)(ka + 512 * (H - 1 - a));
            pa[a] = u32x4{p[0], p[1], p[2], p[3]};
            pb[a] = lds_r128(ua + 512 * a);
        }
        ka -= 64; ua -= 64;
    };
    auto mm = [&](const u32x4* pa, const u32x4* pb) AI {
#pragma unroll
        for (int a = 0; a < H; ++a)
#pragma unroll
            for (int nt = a; nt < H; ++nt)
                if (MASK >> nt & 1) acc[pop_below(MASK, nt)] = mfma16(pa[a], pb[nt - a], acc[pop_below(MASK, nt)]);
#pragma unroll
        for (int i = 0; i < NM; ++i) {
            __builtin_amdgcn_sched_group_barrier(0x008, 1, 0);
            __builtin_amdgcn_sched_group_barrier(0x002, (3 * H + NM - 1) / NM, 0);
            __builtin_amdgcn_sched_group_barrier(0x100, (3 * H + NM - 1) / NM, 0);
        }
        WAIT_LGKM0();
        SB();
    };
    ld(fa[0], fb[0]);
    WAIT_LGKM0();
    SB();
#pragma unroll 1
    for (int b = 0; b < 4; ++b) {
        ld(fa[1], fb[1]);
        mm(fa[0], fb[0]);
        if (b == 3) { ka += 64; ua += 64; }
        ld(fa[0], fb[0]);
        mm(fa[1], fb[1]);
    }
    MFMA_SETTLE();
#pragma unroll
    for (int nt = 0; nt < 8; ++nt)
        if (MASK >> nt & 1) {
            const f32x4 c = acc[pop_below(MASK, nt)];
            const u32x2 uu = *(LAS u32x2*)(ub + 480 + nt * 512 + r16 * 32 + q * 8);
            yo[pop_below(MASK, nt)][0] = pack2(rbf(c[0]) + rbf(bf_lo(uu[0]) * ds), rbf(c[1]) + rbf(bf_hi(uu[0]) * ds));
            yo[pop_below(MASK, nt)][1] = pack2(rbf(c[2]) + rbf(bf_lo(uu[1]) * ds), rbf(c[3]) + rbf(bf_hi(uu[1]) * ds));
        }
}

struct MainArgs {
    const u16* x; const u32* dskip; const char* w1; const char* b1; const char* w2; const u16* b2; char* ws; u16* out;
};

template <int NT>
DI void main_body(const MainArgs& p, u32 sb, int w, int lane, int bid) {
    STS(0);
    constexpr bool GA = NT == 5;
    constexpr int SLOT = NT * 1024;
    char* const ws = p.ws;
    const int r16 = lane & 15, q = lane >> 4;
#define XM 0
    const int xg = bid & 7, xi = bid >> 3;
    const int s = ((xg & ((8 >> XM) - 1)) << XM) | (xi & ((1 << XM) - 1));
    const int mtile = (xg >> (3 - XM)) * (32 >> XM) + (xi >> XM);
    const int g = GA ? w : w + 1;
    const u32 ring = sb + (GA ? L_RINGA + w * 10240 : L_RINGB + (w - 4) * 8192);
    const int prow = lane >> 3, pch = (lane & 7) ^ (((lane >> 4) & 3) << 1), xr = lane >> 1;
    const auto wsr = __builtin_amdgcn_make_buffer_rsrc((void*)ws, 0, 0x7fffffff, 0x00020000);
    GAS u32* const flg = (GAS u32*)(ws + WS_FLAG);
    const int cb = xg * 32 + xi;

    const char* gp = p.w1 + s * 1152 + g * 128;
    u32 vo[5];
    vo[0] = (u32)((8 * (prow >> 2) + (prow & 3)) * 9216 + pch * 16);
    vo[1] = vo[0] + 16 * 9216 - 1024; vo[2] = vo[0] + 4 * 9216 - 2048; vo[3] = vo[0] + 20 * 9216 - 3072;
    vo[4] = (u32)((8 * ((xr >> 2) & 3) + 4 * (xr >> 4) + (xr & 3)) * 9216 + 512 - w * 96 + (lane & 1) * 16);
    int adv = 32 * 9216;
    auto piece = [&](int slot, int i) AI {
        if (i == 0) glds16(gp, vo[0], ring + slot * SLOT);
        if (i == 1) glds16_next<1024>(gp, vo[1]);
        if (i == 2) glds16_next<2048>(gp, vo[2]);
        if (i == 3) glds16_next<3072>(gp, vo[3]);
        if (i == 4) glds16(gp, vo[4], ring + slot * SLOT + 4096);
        if (i == NT - 1) gp += adv;
    };
    auto stage = [&](int slot) AI {
#pragma unroll
        for (int i = 0; i < NT; ++i) piece(slot, i);
    };

#define PRIO_B 0
    u32x4 yw[3];
    int ya[3];
    const bool third = (xi & 3) == 0;
    if (GA) {
        if (w == 0 && lane < 16) lds_w32(sb + L_CNT + lane * 4, 0u);
        WAIT_LGKM0();
        __builtin_amdgcn_s_barrier();
        CFENCE();
        if (w == 1) glds16(p.b1 + s * 1152, lane * 16, sb + L_BIAS);
        if (w == 2 && lane < 8) glds16(p.b1 + s * 1152 + 1024, lane * 16, sb + L_BIAS + 1024);
        stage(0);
        const char* xa = ws + WS_XNA + (size_t)mtile * A_BYTES + w * 18432;
#pragma unroll
        for (int i = 0; i < 18; ++i) glds16(xa + i * 1024, lane * 16, sb + w * 18432 + i * 1024);
        stage(1);
        WAIT_VM(NT);
        arrive(sb, C_X, lane);
        STS(1);
    } else {
        const int wb = w - 4;
        const int chC = 512 + xg * 8 + (xi >> 2), nch = third ? 3 : 2;
        const int c0 = wb == 0 ? 72 : wb == 1 ? 63 : wb == 2 ? 47 : 0, cn = wb == 0 ? 9 : wb == 1 ? 10 : wb == 2 ? 18 : 49;
#pragma unroll 1
        for (int c = 0; c < nch; ++c) {
            const int d = c < 2 ? cb * 2 + c : chC;
            if (lane < cn) glds16(ws + WS_KC + (size_t)d * KC_STRIDE + c0 * 16, lane * 16, sb + L_KC + c * KC_STRIDE + c0 * 16);
            glds16(ws + WS_UT + (size_t)d * 32 + (size_t)(wb * 32) * (DM * 32), (lane >> 1) * (DM * 32) + (lane & 1) * 16,
                   sb + L_CONV + c * CV_LDS + 480 + wb * 1024);
        }
        __builtin_amdgcn_s_barrier();
        CFENCE();
        if (wb < 3) {
            const u32 cbase = sb + L_CONV + wb * CV_LDS;
            if (lane < 30) lds_w128(cbase + lane * 16, u32x4{0, 0, 0, 0});
            else if (lane < 34) lds_w128(cbase + 4608 + 4096 + (lane - 30) * 16, u32x4{0, 0, 0, 0});
            else if (lane < 38) lds_w128(cbase + 4608 + KR_ODD + 4096 + (lane - 34) * 16, u32x4{0, 0, 0, 0});
        }
        const int ci = wb * 64 + lane;
        u32 ia[9];
#pragma unroll
        for (int j = 0; j < 9; ++j) {
            const int l = 2047 - 8 * ci - j;
            const int code = (int)(pack2((float)l, 0.f) & 0xffffu) - 17024;
            ia[j] = sb + L_KC + 2 * (l < 128 ? (l < 0 ? 0 : l) : code);
        }
        const u32 dsa = ((const CAS u32*)p.dskip)[cb], dsc = ((const CAS u32*)p.dskip)[chC >> 1];
        WAIT_VM(0);
#pragma unroll 1
        for (int c = 0; c < nch; ++c) {
            u32 e[9];
#pragma unroll
            for (int j = 0; j < 9; ++j) e[j] = *(LAS u16*)(ia[j] + c * KC_STRIDE);
            if (ci == 255) e[8] = 0;
            const u32 kb = sb + L_CONV + c * CV_LDS + 4608 + ci * 16;
            lds_w128(kb, u32x4{e[0] | e[1] << 16, e[2] | e[3] << 16, e[4] | e[5] << 16, e[6] | e[7] << 16});
            lds_w128(kb + KR_ODD, u32x4{e[1] | e[2] << 16, e[3] | e[4] << 16, e[5] | e[6] << 16, e[7] | e[8] << 16});
        }
        arrive(sb, C_KE, lane);
        await(sb, C_KE, 4);
        STS(1);
        __builtin_amdgcn_s_setprio(3);
        u32x2 yo[4];
        auto widen = [&](u32x2 a, u32x2 b) AI {
            const auto r0 = __builtin_amdgcn_permlane16_swap(a[0], b[0], false, false);
            const auto r1 = __builtin_amdgcn_permlane16_swap(a[1], b[1], false, false);
            return u32x4{r0[0], r1[0], r0[1], r1[1]};
        };
        {
            const u32 ub = sb + L_CONV + (wb >> 1) * CV_LDS;
            const float dsk = (wb >> 1) ? bf_hi(dsa) : bf_lo(dsa);
            int ymask;
            if (wb & 1) { conv_part<0x66>(ub, dsk, lane, yo); ymask = 0x6521; }
            else { conv_part<0x99>(ub, dsk, lane, yo); ymask = 0x7430; }
            const int yoff = (int)WS_YB + (r16 * DM + cb * 2 + (wb >> 1)) * 32 + (q >> 1) * 16;
            yw[0] = widen(yo[0], yo[1]); yw[1] = widen(yo[2], yo[3]);
            ya[0] = yoff + ((ymask >> (4 * (q & 1))) & 15) * (16 * DM * 32);
            ya[1] = yoff + ((ymask >> (8 + 4 * (q & 1))) & 15) * (16 * DM * 32);
        }
        if (third) {
            const u32 ub = sb + L_CONV + 2 * CV_LDS;
            const float dsk = (chC & 1) ? bf_hi(dsc) : bf_lo(dsc);
            if (wb == 0) conv_part<0x81>(ub, dsk, lane, yo);
            else if (wb == 1) conv_part<0x42>(ub, dsk, lane, yo);
            else if (wb == 2) conv_part<0x24>(ub, dsk, lane, yo);
            else conv_part<0x18>(ub, dsk, lane, yo);
            yw[2] = widen(yo[0], yo[1]);
            ya[2] = (int)WS_YB + (r16 * DM + chC) * 32 + (q >> 1) * 16 + ((q & 1) ? 7 - wb : wb) * (16 * DM * 32);
        }
        __builtin_amdgcn_s_setprio(PRIO_B);
        arrive(sb, C_CV, lane);
        await(sb, C_CV, 4);
        stage(0);
        stage(1);
    }
    await(sb, C_X, 4);
    STS(2);

    f32x4 acc[NT][4];
#pragma unroll
    for (int t = 0; t < NT; ++t) {
        const u32x2 bv = *(LAS u32x2*)(sb + L_BIAS + q * 8 + (t < 4 ? g * 128 + t * 32 : 512 + w * 32));
#pragma unroll
        for (int m = 0; m < 4; ++m) acc[t][m] = f32x4{bf_lo(bv[0]), bf_hi(bv[0]), bf_lo(bv[1]), bf_hi(bv[1])};
    }
    const int trow = 4 * q + (r16 >> 2);
    const u32 ta = ring + trow * 128 + 32 * ((trow >> 1) & 3) + 8 * (r16 & 3);
    const u32 tx = ring + 4096 + trow * 32 + 8 * (r16 & 3);

#pragma unroll 1
    for (int ph = 0; ph < 2; ++ph) {
        u32 aa = sb + lane * 16;
        u32x4 fw[2][NT], fx[2][4];
        auto rdw = [&](int b, int t) AI {
            const u32 a0 = t < 4 ? (ta ^ (t << 5)) + b * SLOT : tx + b * SLOT;
            const u32x2 lo = tr_read(a0), hi = tr_read(a0 + (t < 4 ? 2048 : 512));
            fw[b][t] = u32x4{lo[0], lo[1], hi[0], hi[1]};
        };
        auto rdx = [&](int b, int m) AI { fx[b][m] = lds_r128(aa + m * 18432); if (m == 3) aa += 1024; };
        auto rd = [&](int b) AI {
#pragma unroll
            for (int t = 0; t < NT; ++t) rdw(b, t);
#pragma unroll
            for (int m = 0; m < 4; ++m) rdx(b, m);
        };
        auto mf = [&](int b, int i) AI { SB(); acc[i >> 2][i & 3] = mfma16(fw[b][i >> 2], fx[b][i & 3], acc[i >> 2][i & 3]); SB(); };
        auto step = [&](int b, bool st) AI {
#pragma unroll
            for (int i = 0; i < 4 * NT; ++i) {
                mf(b, i);
                if (i < NT) rdw(b ^ 1, i);
                else if (i < NT + 4) rdx(b ^ 1, i - NT);
                constexpr int G0 = GA ? 11 : 9;
                if (i == G0 - 1) { CFENCE(); __builtin_amdgcn_s_waitcnt(0xC47F); CFENCE(); }
                if (i == G0 + 3) __builtin_amdgcn_s_waitcnt(0xC07F);
                if (i >= G0 && ((i - G0) & 1) == 0 && st) piece(b ^ 1, (i - G0) >> 1);
            }
        };
        WAIT_VM(NT);
        rd(0);
        WAIT_LGKM0();
        stage(0);
        WAIT_VM(NT);
        step(0, true);
#pragma unroll 1
        for (int it = 0; it < 7; ++it) {
            if (it == 4) {
                if (ph == 0) arrive(sb, C_1A, lane);
                else {
                    if (!GA) { if (arrive(sb, C_Y, lane) == 3 && lane == 0) __hip_atomic_store(flg + cb * (FLAG_STRIDE / 4), 1u, RLX_AGENT); }
                    await(sb, C_HB, 4);
                }
            }
            WAIT_VM(NT);
            step(1, true);
            WAIT_VM(NT);
            step(0, true);
        }
        if (ph == 0) {
            gp = p.w2 + (size_t)s * 576 * 1152 + g * 128;
            vo[0] = (u32)(prow * 1152 + pch * 16);
            vo[1] = vo[0] + 8 * 1152 - 1024; vo[2] = vo[0] + 16 * 1152 - 2048; vo[3] = vo[0] + 24 * 1152 - 3072;
            vo[4] = (u32)(xr * 1152 + 512 - w * 96 + (lane & 1) * 16);
            adv = 32 * 1152;
        }
        WAIT_VM(NT);
        step(1, ph == 0);
        if (ph == 0) WAIT_VM(NT); else WAIT_VM(0);
        step(0, ph == 0);
        if (ph == 0) arrive(sb, C_1B, lane);
#pragma unroll
        for (int i = 0; i < 4 * NT; ++i) mf(1, i);
        MFMA_SETTLE();
        if (ph == 1) break;
        STS(3);
        if (!GA) {
            __builtin_amdgcn_raw_buffer_store_b128(yw[0], wsr, ya[0], 0, 16);
            __builtin_amdgcn_raw_buffer_store_b128(yw[1], wsr, ya[1], 0, 16);
            if (third) __builtin_amdgcn_raw_buffer_store_b128(yw[2], wsr, ya[2], 0, 16);
        }
        u32x2 hp[NT][4];
#pragma unroll
        for (int t = 0; t < NT; ++t)
#pragma unroll
            for (int m = 0; m < 4; ++m) {
                hp[t][m][0] = pack2(gelu(acc[t][m][0]), gelu(acc[t][m][1]));
                hp[t][m][1] = pack2(gelu(acc[t][m][2]), gelu(acc[t][m][3]));
                acc[t][m] = f32x4{0.f, 0.f, 0.f, 0.f};
            }
#pragma unroll
        for (int t = 0; t < NT; ++t) asm volatile("" : "+v"(hp[t][0]), "+v"(hp[t][1]), "+v"(hp[t][2]), "+v"(hp[t][3]));
        await(sb, GA ? C_1A : C_1B, 8);
        const u32 ha = sb + lane * 16;
#pragma unroll
        for (int m = 0; m < 4; ++m) {
            lds_w128(ha + (m * 18 + 2 * g) * 1024, u32x4{hp[0][m][0], hp[0][m][1], hp[1][m][0], hp[1][m][1]});
            lds_w128(ha + (m * 18 + 2 * g + 1) * 1024, u32x4{hp[2][m][0], hp[2][m][1], hp[3][m][0], hp[3][m][1]});
            if (GA) lds_w64(ha + (m * 18 + 8 + (w >> 1)) * 1024 + (w & 1) * 8, hp[NT - 1][m]);
        }
        arrive(sb, GA ? C_HA : C_HB, lane);
        await(sb, C_HA, 4);
        STS(4);
    }
    STS(5);
    {
        const int po = (int)WS_PART + (mtile * 8 + s) * A_BYTES + lane * 16;
#pragma unroll
        for (int m = 0; m < 4; ++m) {
#pragma unroll
            for (int h = 0; h < 2; ++h) {
                const u32x4 o = {pack2(acc[2*h][m][0], acc[2*h][m][1]), pack2(acc[2*h][m][2], acc[2*h][m][3]),
                                 pack2(acc[2*h+1][m][0], acc[2*h+1][m][1]), pack2(acc[2*h+1][m][2], acc[2*h+1][m][3])};
                __builtin_amdgcn_raw_buffer_store_b128(o, wsr, po, (m * 18 + 2 * g + h) * 1024, 16);
            }
            if (GA) {
                const u32x2 o = {pack2(acc[NT-1][m][0], acc[NT-1][m][1]), pack2(acc[NT-1][m][2], acc[NT-1][m][3])};
                __builtin_amdgcn_raw_buffer_store_b64(o, wsr, po, (m * 18 + 8 + (w >> 1)) * 1024 + (w & 1) * 8, 16);
            }
        }
    }
    WAIT_VM(0);
    const int pf0 = (GA ? 256 : 512) + mtile * 8;
    if (arrive(sb, GA ? C_PA : C_PB, lane) == 3 && lane == 0) __hip_atomic_store(flg + (pf0 + s) * (FLAG_STRIDE / 4), 1u, RLX_AGENT);
    const int tt = mtile * 4 + (s >> 1);
#pragma unroll 1
    for (int c = (w & 3); c < NT; c += 4) {
        const int pr = GA ? (s & 1) * 5 + c : 10 + (s & 1) * 4 + c;
        const int d0 = pr * 32 + q * 4;
        const size_t ro = (size_t)(tt * 16 + r16) * DM + d0;
        const u32x2 xa = *(const u32x2*)(p.x + ro), xb = *(const u32x2*)(p.x + ro + 16);
        const u32x2 ba = *(const u32x2*)(p.b2 + d0), bb = *(const u32x2*)(p.b2 + d0 + 16);
        const int dl = pr * 32 + (lane & 31);
        const int fy = dl < 512 ? dl >> 1 : ((dl - 512) >> 3) * 32 + ((dl - 512) & 7) * 4;
#pragma unroll 1
        for (int spin = 0; spin < SPIN_CAP; ++spin) {
            const u32 f = __hip_atomic_load(flg + fy * (FLAG_STRIDE / 4), RLX_AGENT);
            if (__builtin_amdgcn_ballot_w64(f != 1u) == 0) break;
            __builtin_amdgcn_s_sleep(8);
        }
        CFENCE();
        const u32x4 yv = __builtin_amdgcn_raw_buffer_load_b128(wsr, (int)WS_YB + (tt * DM + pr * 32) * 32 + lane * 16, 0, 16);
#pragma unroll 1
        for (int spin = 0; spin < SPIN_CAP; ++spin) {
            const u32 f = __hip_atomic_load(flg + (pf0 + (lane & 7)) * (FLAG_STRIDE / 4), RLX_AGENT);
            if (__builtin_amdgcn_ballot_w64(f != 1u) == 0) break;
            __builtin_amdgcn_s_sleep(GA ? 8 : 2);
        }
        CFENCE();
        if (c < 4) STS(6);
        const int po = (int)WS_PART + mtile * 8 * A_BYTES + ((s >> 1) * 18 + pr) * 1024 + lane * 16;
        u32x4 pt[8];
#pragma unroll
        for (int i = 0; i < 8; ++i) pt[i] = __builtin_amdgcn_raw_buffer_load_b128(wsr, po, i * A_BYTES, 16);
        lds_w128(ring + lane * 16, yv);
        const u32x2 ya = tr_read(ring + trow * 32 + 8 * (r16 & 3)), yb = tr_read(ring + 512 + trow * 32 + 8 * (r16 & 3));
        float o[8];
#pragma unroll
        for (int i = 0; i < 8; ++i) {
            float sum = 0.f;
#pragma unroll
            for (int k = 0; k < 8; ++k) sum += (i & 1) ? bf_hi(pt[k][i >> 1]) : bf_lo(pt[k][i >> 1]);
            const int j = (i >> 1) & 1;
            const u32 xw = i < 4 ? xa[j] : xb[j], bw = i < 4 ? ba[j] : bb[j], yw = i < 4 ? ya[j] : yb[j];
            const float xv = (i & 1) ? bf_hi(xw) : bf_lo(xw), bv = (i & 1) ? bf_hi(bw) : bf_lo(bw), yy = (i & 1) ? bf_hi(yw) : bf_lo(yw);
            o[i] = rbf(xv + yy) + rbf(rbf(sum) + bv);
        }
        *(u32x2*)(p.out + ro) = u32x2{pack2(o[0], o[1]), pack2(o[2], o[3])};
        *(u32x2*)(p.out + ro + 16) = u32x2{pack2(o[4], o[5]), pack2(o[6], o[7])};
    }
}

__global__ __launch_bounds__(512, 2) void main_kernel(MainArgs p) {
    extern __shared__ __attribute__((aligned(1024))) char dsm[];
    const u32 sb = (u32)(size_t)(LAS char*)dsm;
    const int tid = __builtin_amdgcn_workitem_id_x();
    const int lane = tid & 63, w = __builtin_amdgcn_readfirstlane(tid >> 6);
    const int bid = __builtin_amdgcn_workgroup_id_x();
    if (w < 4) main_body<5>(p, sb, w, lane, bid);
    else main_body<4>(p, sb, w, lane, bid);
}
#if defined(__HIP_DEVICE_COMPILE__)
#pragma clang attribute pop
#endif

#define KMASK 3
extern "C" void kernel_launch(void* const* d_in, const int* in_sizes, int n_in, void* d_out, int out_size,
                              void* d_ws, size_t ws_size, hipStream_t stream) {
    char* ws = (char*)d_ws;
    (void)hipFuncSetAttribute((const void*)main_kernel, hipFuncAttributeMaxDynamicSharedMemorySize, MAIN_LDS);
    PrepArgs pa{(const u16*)d_in[0], (const u16*)d_in[1], (const u16*)d_in[2], (const u16*)d_in[3], (const u16*)d_in[4],
                (const u16*)d_in[5], (const u16*)d_in[6], (const u16*)d_in[7], ws};
    if (KMASK & 1) prep_kernel<<<128 + DM / 2, 576, 0, stream>>>(pa);
    MainArgs a{(const u16*)d_in[0], (const u32*)d_in[8], (const char*)d_in[10], (const char*)d_in[11], (const char*)d_in[12],
               (const u16*)d_in[13], ws, (u16*)d_out};
    if (KMASK & 2) main_kernel<<<256, 512, MAIN_LDS, stream>>>(a);
}
```

```cpp
#include <hip/hip_runtime.h>
#include <stdint.h>

typedef unsigned short u16;
typedef unsigned int u32;
typedef __attribute__((ext_vector_type(2))) __bf16 bf16x2;
typedef __attribute__((ext_vector_type(8))) __bf16 bf16x8;
typedef __attribute__((ext_vector_type(4))) short s16x4;
typedef __attribute__((ext_vector_type(2))) float f32x2;
typedef __attribute__((ext_vector_type(4))) float f32x4;
typedef __attribute__((ext_vector_type(2))) unsigned u32x2;
typedef __attribute__((ext_vector_type(4))) unsigned u32x4;
#define LAS __attribute__((address_space(3)))
#define GAS __attribute__((address_space(1)))
#define CAS __attribute__((address_space(4)))
#define DI __device__ __forceinline__
#define AI __attribute__((always_inline))
#if defined(__HIP_DEVICE_COMPILE__)
#pragma clang attribute push(__attribute__((target("no-packed-fp32-ops"))), apply_to = function)
#endif
#define SB() __builtin_amdgcn_sched_barrier(0)
#define CFENCE() asm volatile("" ::: "memory")
#define MFMA_SETTLE() do { SB(); asm volatile("s_nop 15\n\ts_nop 15"); SB(); } while (0)
#define WAIT_VM(n) asm volatile("s_waitcnt vmcnt(%0)" :: "i"(n) : "memory")
#define WAIT_LGKM0() do { CFENCE(); __builtin_amdgcn_s_waitcnt(0xC07F); CFENCE(); } while (0)
#define RLX_AGENT __ATOMIC_RELAXED, __HIP_MEMORY_SCOPE_AGENT
#define RLX_WG __ATOMIC_RELAXED, __HIP_MEMORY_SCOPE_WORKGROUP

#define XM 1
#define PACE 4
#define PRIO_C1 1
#define PRIO_C2 0
#define UNAP 32
#define GNAP 16
#define CBAR 1
#define DEFER 99
#define KWAIT 0
#define CDELAY 0

constexpr int SEQ = 2048, DM = 576, NST = 128;
constexpr int FLAG_STRIDE = 128, FW = FLAG_STRIDE / 4;
constexpr int F_U = 0, F_Y = 1, F_P = 2, F_NONCE = 256;
constexpr int WS_FLAG = 0;
constexpr int WS_GO = 512 * FLAG_STRIDE;
constexpr int WS_UT = WS_GO + 1024;
constexpr int WS_YB = WS_UT + SEQ * DM * 2;
constexpr int WS_YC = WS_YB + SEQ * 512 * 2;
constexpr int WS_PART = WS_YB + SEQ * DM * 2;

constexpr int A_BYTES = 73728;
constexpr int L_RINGA = A_BYTES, L_RINGB = L_RINGA + 40960, L_C = L_RINGB + 32768;
constexpr int KR_ODD = 4160, CV_LDS = 4608 + 8320, L_KC = L_C + CV_LDS;
constexpr int KG_ROW = 272, KG_IMG = 4608;
constexpr int L_AX = L_RINGA, L_CV1 = L_RINGA + 12288;
constexpr int L_BIAS = 161792, L_CNT = L_BIAS + 1152, L_SINK = L_CNT + 128;
constexpr int MAIN_LDS = 163840;
constexpr int SPIN_CAP = 1 << 17;
enum { C_S = 0, C_XN = 4, C_W, C_1A, C_1B, C_HA, C_HB, C_F, C_C, C_E, C_G, C_B2, C_P };

DI float bf_lo(u32 v) { return __builtin_bit_cast(float, v << 16); }
DI float bf_hi(u32 v) { return __builtin_bit_cast(float, v & 0xffff0000u); }
DI u32 pack2(float lo, float hi) {
    f32x2 v = {lo, hi};
    return __builtin_bit_cast(u32, __builtin_convertvector(v, bf16x2));
}
DI u32x2 pack4(f32x4 c) { return u32x2{pack2(c[0], c[1]), pack2(c[2], c[3])}; }
DI float rbf(float f) { return bf_lo(pack2(f, f)); }
DI f32x4 mfma16(u32x4 a, u32x4 b, f32x4 c) {
    return __builtin_amdgcn_mfma_f32_16x16x32_bf16(__builtin_bit_cast(bf16x8, a), __builtin_bit_cast(bf16x8, b), c, 0, 0, 0);
}
DI void mfma16_acc(f32x4& c, u32x4 a, u32x4 b) {
    asm volatile("v_mfma_f32_16x16x32_bf16 %0, %1, %2, %0" : "+v"(c) : "v"(a), "v"(b));
}
DI void mfma16_set(f32x4& d, u32x4 a, u32x4 b, f32x4 c) {
    asm volatile("v_mfma_f32_16x16x32_bf16 %0, %1, %2, %3" : "=&v"(d) : "v"(a), "v"(b), "v"(c));
}
DI void mfma16_zero(f32x4& d, u32x4 a, u32x4 b) {
    asm volatile("v_mfma_f32_16x16x32_bf16 %0, %1, %2, 0" : "=&v"(d) : "v"(a), "v"(b));
}
DI u32x2 tr_read(u32 addr) {
    return __builtin_bit_cast(u32x2, __builtin_amdgcn_ds_read_tr16_b64_v4i16((LAS s16x4*)addr));
}
DI u32x4 lds_r128(u32 addr) { return *(LAS u32x4*)addr; }
DI void lds_w128(u32 addr, u32x4 v) { *(LAS u32x4*)addr = v; }
DI void lds_w64(u32 addr, u32x2 v) { *(LAS u32x2*)addr = v; }
DI void lds_w32(u32 addr, u32 v) { *(LAS u32*)addr = v; }
DI f32x4 lds_rf4(u32 addr) { return *(LAS f32x4*)addr; }
DI void lds_wf4(u32 addr, f32x4 v) { *(LAS f32x4*)addr = v; }
DI int fresh_lane() {
    int l;
    asm volatile("v_mbcnt_lo_u32_b32 %0, -1, 0\n\tv_mbcnt_hi_u32_b32 %0, -1, %0" : "=v"(l));
    return l;
}
DI void glds16(const char* sbase, u32 voff, u32 lds) {
    asm volatile("s_mov_b32 m0, %2\n\ts_nop 0\n\tglobal_load_lds_dwordx4 %0, %1" :: "v"(voff), "s"(sbase), "s"(lds) : "memory");
}
template <int OFF>
DI void glds16_next(const char* sbase, u32 voff) {
    asm volatile("global_load_lds_dwordx4 %0, %1 offset:%2" :: "v"(voff), "s"(sbase), "i"(OFF) : "memory");
}
DI void touch(const char* sbase, u32 voff, u32 lds) {
    asm volatile("s_mov_b32 m0, %2\n\ts_nop 0\n\tglobal_load_lds_dword %0, %1" :: "v"(voff), "s"(sbase), "s"(lds) : "memory");
}
template <class R>
DI void st128(u32x4 v, R rsrc, int voff, int soff) {
    __builtin_amdgcn_raw_buffer_store_b128(v, rsrc, voff, soff, 16);
    SB(); asm volatile("s_nop 1"); SB();
}
DI u32x4 widen(u32x2 a, u32x2 b) {
    const auto r0 = __builtin_amdgcn_permlane16_swap(a[0], b[0], false, false);
    const auto r1 = __builtin_amdgcn_permlane16_swap(a[1], b[1], false, false);
    return u32x4{r0[0], r1[0], r0[1], r1[1]};
}
DI float gelu(float v) {
    const float c0 = -2.f * 0.7978845608028654f * 1.4426950408889634f, c1 = c0 * 0.044715f;
    const float e = __builtin_amdgcn_exp2f(v * __builtin_fmaf(v * v, c1, c0));
    return v * __builtin_amdgcn_rcpf(1.f + e);
}
struct cpx { float re, im; };
DI cpx cmul(cpx a, cpx b) { return cpx{a.re * b.re - a.im * b.im, a.re * b.im + a.im * b.re}; }

DI void arrive(u32 sb, int c) {
    unsigned long long ex;
    asm volatile("s_mov_b64 %0, exec\n\ts_mov_b64 exec, 1\n\tds_add_u32 %1, %2\n\ts_mov_b64 exec, %0"
                 : "=&s"(ex) : "v"(sb + L_CNT + c * 4), "v"(1u) : "memory");
}
DI u32 arrive_rtn(u32 sb, int c) {
    unsigned long long ex;
    u32 r;
    asm volatile("s_mov_b64 %0, exec\n\ts_mov_b64 exec, 1\n\tds_add_rtn_u32 %1, %2, %3\n\ts_mov_b64 exec, %0\n\ts_waitcnt lgkmcnt(0)"
                 : "=&s"(ex), "=&v"(r) : "v"(sb + L_CNT + c * 4), "v"(1u) : "memory");
    return (u32)__builtin_amdgcn_readfirstlane(r);
}
template <int NAP = 1>
DI void await(u32 sb, int c, u32 n) {
    CFENCE();
#pragma unroll 1
    for (int i = 0; i < SPIN_CAP; ++i) {
        if ((u32)__builtin_amdgcn_readfirstlane(__hip_atomic_load((LAS u32*)(sb + L_CNT + c * 4), RLX_WG)) >= n) break;
        __builtin_amdgcn_s_sleep(NAP);
    }
    CFENCE();
}
DI void poll(GAS u32* f, bool nap) {
#pragma unroll 1
    for (int spin = 0; spin < SPIN_CAP; ++spin) {
        if (__builtin_amdgcn_ballot_w64(__hip_atomic_load(f, RLX_AGENT) != 1u) == 0) break;
        if (nap) __builtin_amdgcn_s_sleep(8); else __builtin_amdgcn_s_sleep(2);
    }
    CFENCE();
}

#define TL()
#define TLEND()

struct MainArgs {
    const u16 *x, *Are, *Aim, *Bre, *Bim, *Cre, *Cim;
    const u32 *logdt, *dskip;
    const char *w1, *b1, *w2;
    const u16* b2;
    char* ws;
    u16* out;
};
struct Ctx {
    u32 sb;
    int w, xi, s, mtile, wg;
};

template <bool W1>
DI void stage_offsets(u32* vo, int lane, int w) {
    constexpr int RS = W1 ? 9216 : 1152;
    const int prow = lane >> 3, pch = (lane & 7) ^ (((lane >> 4) & 3) << 1), xr = lane >> 1;
    vo[0] = (u32)((W1 ? 8 * (prow >> 2) + (prow & 3) : prow) * RS + pch * 16);
    vo[1] = vo[0] + (W1 ? 16 : 8) * RS - 1024; vo[2] = vo[0] + (W1 ? 4 : 16) * RS - 2048; vo[3] = vo[0] + (W1 ? 20 : 24) * RS - 3072;
    vo[4] = (u32)((W1 ? 8 * ((xr >> 2) & 3) + 4 * (xr >> 4) + (xr & 3) : xr) * RS + 512 - w * 96 + (lane & 1) * 16);
}
template <int NT>
DI void stage_piece(const char*& gp, const u32* vo, u32 ring, int adv, int slot, int i) {
    if (i == 0) glds16(gp, vo[0], ring + slot * NT * 1024);
    if (i == 1) glds16_next<1024>(gp, vo[1]);
    if (i == 2) glds16_next<2048>(gp, vo[2]);
    if (i == 3) glds16_next<3072>(gp, vo[3]);
    if (i == 4) glds16(gp, vo[4], ring + slot * NT * 1024 + 4096);
    if (i == NT - 1) gp += adv;
}

struct Side { u32x2 xa, xb, ba, bb; };
DI Side load_side(const MainArgs& p, Ctx& cx, int pr) {
    const int lane = fresh_lane(), d0 = pr * 32 + (lane >> 4) * 4;
    const u16* xp = p.x + (size_t)((cx.mtile * 4 + (cx.s >> 1)) * 16 + (lane & 15)) * DM + d0;
    return Side{*(const u32x2*)xp, *(const u32x2*)(xp + 16), *(const u32x2*)(p.b2 + d0), *(const u32x2*)(p.b2 + d0 + 16)};
}
DI void reduce_chunk(const MainArgs& p, Ctx& cx, const Side& sd, int pr, int fp, u32 ys, bool nap) {
    const int lane = fresh_lane(), r16 = lane & 15, q = lane >> 4;
    const int tt = cx.mtile * 4 + (cx.s >> 1), nt = tt >> 4;
    const auto wsr = __builtin_amdgcn_make_buffer_rsrc((void*)p.ws, 0, 0x7fffffff, 0x00020000);
    GAS u32* const flg = (GAS u32*)(p.ws + WS_FLAG);
    const size_t ro = (size_t)(tt * 16 + r16) * DM;
    const u32x2 xa = sd.xa, xb = sd.xb, ba = sd.ba, bb = sd.bb;
    const int dl = pr * 32 + (lane & 31);
    poll(flg + (lane < 32 ? F_Y + (pr < 16 ? dl >> 1 : (dl - 512) * 4 + (nt < 4 ? nt : 7 - nt)) * FW : fp + (cx.mtile * 8 + (lane & 7)) * FW), nap);
    const int yo = pr < 16 ? WS_YB + (tt * 512 + pr * 32) * 32 + lane * 16 : WS_YC + ((pr - 16) * 32 + (lane >> 1)) * 4096 + tt * 32 + (lane & 1) * 16;
    const u32x4 yv = __builtin_amdgcn_raw_buffer_load_b128(wsr, yo, 0, 16);
    TL();
    const int pi = WS_PART + cx.mtile * 8 * A_BYTES + ((cx.s >> 1) * 18 + pr) * 1024 + lane * 16;
    u32x4 pt[8];
#pragma unroll
    for (int i = 0; i < 8; ++i) pt[i] = __builtin_amdgcn_raw_buffer_load_b128(wsr, pi, i * A_BYTES, 16);
    lds_w128(ys + lane * 16, yv);
    const u32 ta = ys + (4 * q + (r16 >> 2)) * 32 + 8 * (r16 & 3);
    const u32x2 y0 = tr_read(ta), y1 = tr_read(ta + 512);
    float o[8];
#pragma unroll
    for (int i = 0; i < 8; ++i) {
        const int j = (i >> 1) & 1;
        const u32 xw = i < 4 ? xa[j] : xb[j], bw = i < 4 ? ba[j] : bb[j], yz = i < 4 ? y0[j] : y1[j];
        o[i] = (i & 1) ? (bf_hi(xw) + bf_hi(yz)) + bf_hi(bw) : (bf_lo(xw) + bf_lo(yz)) + bf_lo(bw);
    }
#pragma unroll
    for (int k = 0; k < 8; ++k)
#pragma unroll
        for (int i = 0; i < 8; ++i) o[i] += (i & 1) ? bf_hi(pt[k][i >> 1]) : bf_lo(pt[k][i >> 1]);
    *(u32x4*)(p.out + ro + pr * 32 + (q & 1) * 16 + (q >> 1) * 8) =
        widen(u32x2{pack2(o[0], o[1]), pack2(o[2], o[3])}, u32x2{pack2(o[4], o[5]), pack2(o[6], o[7])});
}

template <int NT>
DI void mlp_body(const MainArgs& p, Ctx& cx) {
    constexpr bool GA = NT == 5;
    constexpr int SLOT = NT * 1024, GI = GA ? 0 : 1;
    const u32 sb = cx.sb;
    const int w = cx.w, s = cx.s, wb = w & 3, g = GA ? w : w + 1;
    const u32 ring = sb + (GA ? L_RINGA + w * 10240 : L_RINGB + wb * 8192);
    const char* gp = p.w1 + s * 1152 + g * 128 + 64 * 9216;
    int adv = 32 * 9216;
    u32 vo[5], ta, tx, a0;
    f32x4 acc[NT][4], bias[NT];
    {
        const int lane = fresh_lane(), r16 = lane & 15, q = lane >> 4, trow = 4 * q + (r16 >> 2);
        stage_offsets<true>(vo, lane, w);
        ta = ring + trow * 128 + 32 * ((trow >> 1) & 3) + 8 * (r16 & 3);
        tx = ring + 4096 + trow * 32 + 8 * (r16 & 3);
        a0 = sb + lane * 16;
        await(sb, C_W, 4);
        u32x2 bv[NT];
#pragma unroll
        for (int t = 0; t < NT; ++t) bv[t] = *(LAS u32x2*)(sb + L_BIAS + q * 8 + (t < 4 ? g * 128 + t * 32 : 512 + w * 32));
#pragma unroll
        for (int t = 0; t < NT; ++t) bias[t] = f32x4{bf_lo(bv[t][0]), bf_hi(bv[t][0]), bf_lo(bv[t][1]), bf_hi(bv[t][1])};
#pragma unroll
        for (int t = 0; t < NT; ++t) asm volatile("" : "+v"(bias[t]));
        await(sb, C_XN, 8);
    }
    auto piece = [&](int slot, int i) AI { stage_piece<NT>(gp, vo, ring, adv, slot, i); };
    TL();
#pragma unroll
    for (int ph = 0; ph < 2; ++ph) {
        u32 aa = a0;
        u32x4 fw[2][NT], fx[4];
        auto rdw = [&](int b, int t) AI {
            const u32 a = t < 4 ? (ta ^ (t << 5)) + b * SLOT : tx + b * SLOT;
            const u32x2 lo = tr_read(a), hi = tr_read(a + (t < 4 ? 2048 : 512));
            fw[b][t] = u32x4{lo[0], lo[1], hi[0], hi[1]};
        };
        auto rdx = [&](int m) AI { fx[m] = lds_r128(aa + m * 18432); if (m == 3) aa += 1024; };
        auto mf = [&](int b, int i, bool first) AI {
            SB();
            if (!first) mfma16_acc(acc[i % NT][i / NT], fw[b][i % NT], fx[i / NT]);
            else if (ph == 0) mfma16_set(acc[i % NT][i / NT], fw[b][i % NT], fx[i / NT], bias[i % NT]);
            else mfma16_zero(acc[i % NT][i / NT], fw[b][i % NT], fx[i / NT]);
            SB();
        };
        auto step = [&](int b, bool st, bool first) AI {
#pragma unroll
            for (int i = 0; i < 4 * NT; ++i) {
                mf(b, i, first);
                if (i < NT) rdw(b ^ 1, i);
                if (i % NT == NT - 1) rdx(i / NT);
                constexpr int G0 = NT + 3;
                if (i == G0 - 1) { CFENCE(); __builtin_amdgcn_s_waitcnt(0xC17F); CFENCE(); }
                if (i >= G0 && i < G0 + NT && st) piece(b ^ 1, i - G0);
            }
        };
        WAIT_VM(NT);
#pragma unroll
        for (int t = 0; t < NT; ++t) rdw(0, t);
#pragma unroll
        for (int m = 0; m < 4; ++m) rdx(m);
        WAIT_LGKM0();
#pragma unroll
        for (int i = 0; i < NT; ++i) piece(0, i);
        WAIT_VM(NT);
        step(0, true, true);
#pragma unroll 1
        for (int it = 0; it < 7; ++it) {
            if (it == 4) {
                if (ph == 0) { arrive(sb, C_1A); WAIT_LGKM0(); }
                else await(sb, C_HB, 4);
            }
            WAIT_VM(NT);
            step(1, true, false);
            WAIT_VM(NT);
            step(0, true, false);
        }
        if (ph == 0) {
            gp = p.w2 + (size_t)s * 576 * 1152 + g * 128;
            stage_offsets<false>(vo, fresh_lane(), w);
            adv = 32 * 1152;
        }
        WAIT_VM(NT);
        step(1, ph == 0, false);
        if (ph == 0) WAIT_VM(NT); else WAIT_VM(0);
        step(0, ph == 0, false);
        WAIT_LGKM0();
        if (ph == 0) arrive(sb, C_1B);
        else arrive(sb, GA ? C_G : C_B2);
#pragma unroll
        for (int i = 0; i < 4 * NT; ++i) mf(1, i, false);
        MFMA_SETTLE();
        if (ph == 1) break;
        TL();
        u32x2 hp[NT][4];
#pragma unroll
        for (int t = 0; t < NT; ++t)
#pragma unroll
            for (int m = 0; m < 4; ++m) {
                hp[t][m][0] = pack2(gelu(acc[t][m][0]), gelu(acc[t][m][1]));
                hp[t][m][1] = pack2(gelu(acc[t][m][2]), gelu(acc[t][m][3]));
            }
#pragma unroll
        for (int t = 0; t < NT; ++t) asm volatile("" : "+v"(hp[t][0]), "+v"(hp[t][1]), "+v"(hp[t][2]), "+v"(hp[t][3]));
        TL();
        await(sb, GA ? C_1A : C_1B, 9);
#pragma unroll
        for (int m = 0; m < 4; ++m) {
            lds_w128(a0 + (m * 18 + 2 * g) * 1024, u32x4{hp[0][m][0], hp[0][m][1], hp[1][m][0], hp[1][m][1]});
            lds_w128(a0 + (m * 18 + 2 * g + 1) * 1024, u32x4{hp[2][m][0], hp[2][m][1], hp[3][m][0], hp[3][m][1]});
            if (GA) lds_w64(a0 + (m * 18 + 8 + (w >> 1)) * 1024 + (w & 1) * 8, hp[NT - 1][m]);
        }
        arrive(sb, GA ? C_HA : C_HB);
        await(sb, C_HA, 4);
        TL();
    }
    TL();
    const int pr = (GA ? 0 : 10) + (s & 1) * 4 + wb;
    const Side sd = load_side(p, cx, pr);
    CFENCE();
    {
        const int lane = fresh_lane();
        const auto wsr = __builtin_amdgcn_make_buffer_rsrc((void*)p.ws, 0, 0x7fffffff, 0x00020000);
        const int po = WS_PART + cx.wg * A_BYTES + lane * 16;
#pragma unroll
        for (int i = 0; i < 8; ++i) {
            const int m = i >> 1, h = i & 1;
            const u32x2 lo = pack4(acc[2 * h][m]), hi = pack4(acc[2 * h + 1][m]);
            if (GA && i == DEFER) await<2>(sb, C_B2, 4);
            st128(u32x4{lo[0], lo[1], hi[0], hi[1]}, wsr, po, (m * 18 + 2 * g + h) * 1024);
            if (GA && PACE && i < DEFER) { __builtin_amdgcn_s_sleep(PACE); SB(); }
            if (GA && i == 3) {
                await(sb, C_G, 4);
#pragma unroll
                for (int k = 0; k < 4; ++k) lds_w64(sb + L_AX + ((w >> 1) * 4 + k) * 1024 + lane * 16 + (w & 1) * 8, pack4(acc[NT - 1][k]));
                arrive(sb, C_E);
            }
        }
        if (GA) {
            await(sb, C_E, 4);
#pragma unroll
            for (int i = 0; i < 2; ++i)
                st128(lds_r128(sb + L_AX + (w * 2 + i) * 1024 + lane * 16), wsr, po, (((w & 1) * 2 + i) * 18 + 8 + (w >> 1)) * 1024);
        }
        TL();
        await(sb, C_F, 1);
        WAIT_VM(0);
        TL();
        if (arrive_rtn(sb, C_P + GI) == 3 && lane == 0) __hip_atomic_store((GAS u32*)(p.ws + WS_FLAG) + F_P + GI + cx.wg * FW, 1u, RLX_AGENT);
    }
    if (GA) await<4>(sb, C_P + 2, 4);
    reduce_chunk(p, cx, sd, pr, F_P + GI, GA ? sb + L_AX + 8192 + w * 1024 : ring, GA);
    TLEND();
}

constexpr int hi_bit(int m) { int r = 0; for (int i = 0; i < 8; ++i) if (m >> i & 1) r = i; return r; }
constexpr int pop_below(int m, int b) { int r = 0; for (int i = 0; i < b; ++i) r += m >> i & 1; return r; }
struct ConvSched { int n; int acc[36]; int nt[36]; int a[36]; };
constexpr ConvSched conv_sched(int mask, int split) {
    ConvSched s{};
    int rem[16] = {}, nts[16] = {}, nxt[8] = {}, last[16] = {}, na = 0;
    for (int t = 0; t < 8; ++t) if (mask >> t & 1) {
        const int parts = (split >> t & 1) ? 2 : 1;
        for (int k = 0; k < parts; ++k) { nts[na] = t; rem[na] = (t + 1 + (parts - 1 - k)) / parts; last[na] = -100; ++na; }
        s.n += t + 1;
    }
    for (int i = 0; i < s.n; ++i) {
        int best = -1;
        for (int gap = 4; gap >= 1 && best < 0; --gap)
            for (int k = 0; k < na; ++k)
                if (rem[k] > 0 && i - last[k] >= gap && (best < 0 || rem[k] > rem[best])) best = k;
        s.acc[i] = best; s.nt[i] = nts[best]; s.a[i] = nxt[nts[best]]++;
        --rem[best]; last[best] = i;
    }
    return s;
}
template <int MASK, int SPLIT, int NB>
DI void conv_acc(u32 ub, int b0, int lane, f32x4* out) {
    constexpr int H = hi_bit(MASK) + 1, NA = pop_below(MASK, 8) + pop_below(SPLIT, 8);
    constexpr ConvSched S = conv_sched(MASK, SPLIT);
    const int r16 = lane & 15, q = lane >> 4;
    const u32 k0 = ub + 4608 + ((r16 & 1) ? (2031 + 8 * q - r16) * 2 : KR_ODD + (2030 + 8 * q - r16) * 2) - 64 * b0;
    u32 ka[4], ua = ub + 448 + r16 * 32 + q * 16 - 64 * b0;
#pragma unroll
    for (int i = 0; i < 4; ++i) ka[i] = k0 - 512 - 1024 * i;
    f32x4 acc[NA];
#pragma unroll
    for (int i = 0; i < NA; ++i) { acc[i] = f32x4{0.f, 0.f, 0.f, 0.f}; asm volatile("" : "+v"(acc[i])); }
    SB();
#pragma unroll 1
    for (int b = 0; b < NB; ++b) {
        u32x4 fa[H], fb[H];
#pragma unroll
        for (int a = 0; a < H; ++a) {
            const LAS u32* kp = (const LAS u32*)(ka[a >> 1]) + ((a & 1) ? 0 : 128);
            fa[a] = u32x4{kp[0], kp[1], kp[2], kp[3]};
            fb[H - 1 - a] = lds_r128(ua + 512 * (H - 1 - a));
        }
#pragma unroll
        for (int i = 0; i < (H + 1) / 2; ++i) { ka[i] -= 64; asm volatile("" : "+v"(ka[i])); }
        ua -= 64; asm volatile("" : "+v"(ua));
        SB();
#pragma unroll
        for (int i = 0; i < S.n; ++i) mfma16_acc(acc[S.acc[i]], fa[S.a[i]], fb[S.nt[i] - S.a[i]]);
        SB();
    }
    MFMA_SETTLE();
    int k = 0;
#pragma unroll
    for (int t = 0; t < 8; ++t)
        if (MASK >> t & 1) {
            out[pop_below(MASK, t)] = (SPLIT >> t & 1) ? acc[k] + acc[k + 1] : acc[k];
            k += (SPLIT >> t & 1) ? 2 : 1;
        }
}

DI void ssm_body(const MainArgs& p, Ctx& cx) {
    const u32 sb = cx.sb;
    const int wb = cx.w & 3, s = cx.s, wg = cx.wg, xi = cx.xi;
    const int lane = fresh_lane(), r16 = lane & 15, q = lane >> 4;
    const int chC = 512 + (wg >> 2), qt = wg & 3;
    const auto wsr = __builtin_amdgcn_make_buffer_rsrc((void*)p.ws, 0, 0x7fffffff, 0x00020000);
    GAS u32* const flg = (GAS u32*)(p.ws + WS_FLAG);
    GAS u32* const gow = (GAS u32*)(p.ws + WS_GO);
    constexpr int PER = 5184 / (32 >> XM), NTOUCH = (PER + 255) / 256;
    const int ch = wb < 2 ? wg * 2 + wb : chC;

    __builtin_amdgcn_s_setprio(PRIO_C1);
    u32 prm[6];
    u32 go0;
    {
        const u16* const pp[6] = {p.Are, p.Aim, p.Bre, p.Bim, p.Cre, p.Cim};
        const u32 o = (u32)(ch * NST + lane * 2) * 2;
#pragma unroll
        for (int i = 0; i < 6; ++i) asm volatile("global_load_dword %0, %1, %2" : "=v"(prm[i]) : "v"(o), "s"(pp[i]) : "memory");
        asm volatile("global_load_dword %0, %1, off sc1" : "=v"(go0) : "v"(gow + wg) : "memory");
    }
    const u32 ldw = ((const CAS u32*)p.logdt)[ch >> 1], dsw = ((const CAS u32*)p.dskip)[ch >> 1];
    __builtin_amdgcn_s_sleep(CDELAY);
    {
        u32 vo[5];
        stage_offsets<true>(vo, lane, wb);
        glds16(p.b1 + s * 1152 + (wb & 1) * 128, lane * 16, sb + L_BIAS + (wb & 1) * 128);
        const char* ga = p.w1 + s * 1152 + wb * 128;
        const char* gb = ga + 640;
#pragma unroll
        for (int sl = 0; sl < 2; ++sl) {
#pragma unroll
            for (int i = 0; i < 5; ++i) stage_piece<5>(ga, vo, sb + L_RINGA + wb * 10240, 32 * 9216, sl, i);
#pragma unroll
            for (int i = 0; i < 4; ++i) stage_piece<4>(gb, vo, sb + L_RINGB + wb * 8192, 32 * 9216, sl, i);
            if (sl == CBAR) { __builtin_amdgcn_s_barrier(); CFENCE(); }
        }
#pragma unroll
        for (int k = 0; k < NTOUCH; ++k) {
            const int t = k * 256 + wb * 64 + lane, i = (xi >> XM) * PER + (t < PER ? t : 0), row = i / 9;
            touch(p.w1 + s * 1152, (u32)(row * 9216 + (i - row * 9) * 128), sb + L_SINK);
        }
#pragma unroll
        for (int k = 0; k < NTOUCH; ++k) {
            const int t = k * 256 + wb * 64 + lane;
            touch(p.w2 + (size_t)s * 663552, (u32)((xi >> XM) * PER + (t < PER ? t : 0)) * 128, sb + L_SINK);
        }
    }
    asm volatile("s_waitcnt vmcnt(%7)" : "+v"(prm[0]), "+v"(prm[1]), "+v"(prm[2]), "+v"(prm[3]), "+v"(prm[4]), "+v"(prm[5]), "+v"(go0) : "i"(2 * NTOUCH) : "memory");
    arrive(sb, C_W);
    TL();

    u32x2 kc0, kc1, kc2;
    if (wb < 3) {
        if (KWAIT) await<2>(sb, C_XN, 8);
        const float dt = rbf(__builtin_expf((ch & 1) ? bf_hi(ldw) : bf_lo(ldw)));
        const u32 img = sb + L_C + wb * KG_IMG;
        f32x4 k0 = {0.f, 0.f, 0.f, 0.f}, k1 = k0, k2 = k0;
        if (lane == 0) k0[0] = (ch & 1) ? bf_hi(dsw) : bf_lo(dsw);
        const u32 wa = img + lane * 4, ra = img + r16 * KG_ROW + q * 16;
#pragma unroll 1
        for (int hf = 0; hf < 2; ++hf) {
            float v[6];
#pragma unroll
            for (int i = 0; i < 6; ++i) { v[i] = bf_lo(prm[i]); prm[i] >>= 16; }
            const float a2 = dt * v[0] * 1.4426950408889634f, bt = dt * v[1] * 0.15915494309189535f;
            const cpx c = cmul(cpx{v[4], v[5]}, cpx{dt * v[2], dt * v[3]});
            auto cexp = [&](float l) AI {
                const float e = __builtin_amdgcn_exp2f(a2 * l), ph = __builtin_amdgcn_fractf(bt * l);
                return cpx{e * __builtin_amdgcn_cosf(ph), e * __builtin_amdgcn_sinf(ph)};
            };
            auto csq = [&](cpx a) AI { return cpx{a.re * a.re - a.im * a.im, 2.f * a.re * a.im}; };
            auto put = [&](int row, cpx u) AI { lds_w32(wa + row * KG_ROW, pack2(u.re, u.im)); };
            auto get = [&](u32x4* f) AI {
                CFENCE();
#pragma unroll
                for (int k = 0; k < 4; ++k) f[k] = lds_r128(ra + k * 64);
                CFENCE();
            };
            const cpx e1 = cexp(1.f), e8 = cexp(8.f), e64 = cexp(64.f), e128 = csq(e64), e256 = csq(e128);
            u32x4 fb[4], fa[4];
            {
                const cpx c8 = {e8.re, -e8.im}, c64 = {e64.re, -e64.im};
                cpx u = c8;
                put(0, cpx{1.f, 0.f}); put(8, c64);
#pragma unroll
                for (int i = 1; i < 8; ++i) { put(i, u); put(8 + i, cmul(u, c64)); if (i < 7) u = cmul(u, c8); }
                get(fb);
            }
            cpx ev[4], ew[4];
            {
                cpx u = c;
#pragma unroll
                for (int i = 0; i < 8; ++i) {
                    const cpx u2 = cmul(u, e128);
                    put(i, u); put(8 + i, u2);
                    if ((i & 1) == 0) { ev[i >> 1] = u; ew[i >> 1] = u2; }
                    if (i < 7) u = cmul(u, e1);
                }
                get(fa);
#pragma unroll
                for (int k = 0; k < 4; ++k) k0 = mfma16(fa[k], fb[k], k0);
            }
            cpx t0, t1;
            {
#pragma unroll
                for (int i = 0; i < 4; ++i) {
                    const cpx g1 = cmul(ev[i], e256), g2 = cmul(ew[i], e256);
                    put(i, g1); put(4 + i, g2);
                    if ((i & 1) == 0) {
                        const cpx h1 = cmul(g1, e256), h2 = cmul(g2, e256), h3 = cmul(h1, e256), h4 = cmul(h2, e256);
                        put(8 + (i >> 1), h1); put(10 + (i >> 1), h2); put(12 + (i >> 1), h3); put(14 + (i >> 1), h4);
                        if (i == 0) { t0 = h3; t1 = h4; }
                    }
                }
                get(fa);
#pragma unroll
                for (int k = 0; k < 4; ++k) k1 = mfma16(fa[k], fb[k], k1);
            }
            {
#pragma unroll
                for (int i = 0; i < 5; ++i) {
                    t0 = cmul(t0, e256); put(2 * i, t0);
                    if (i < 4) { t1 = cmul(t1, e256); put(2 * i + 1, t1); }
                }
                get(fa);
#pragma unroll
                for (int k = 0; k < 4; ++k) k2 = mfma16(fb[k], fa[k], k2);
            }
        }
        MFMA_SETTLE();
        kc0 = pack4(k0); kc1 = pack4(k1); kc2 = pack4(k2);
    } else {
        const bool master = wg == 0;
        const u32 nonce = (u32)__builtin_amdgcn_readfirstlane(go0) + 1u;
        if (lane == 0) __hip_atomic_store(flg + (F_NONCE + wg) * FW, nonce, RLX_AGENT);
        if (master) {
#pragma unroll 1
            for (int i = 0; i < 4; ++i) st128(u32x4{0, 0, 0, 0}, wsr, WS_FLAG + (i * 64 + lane) * FLAG_STRIDE, 0);
        }
        auto echo = [&]() AI {
            u32x4 n;
#pragma unroll
            for (int i = 0; i < 4; ++i) n[i] = __hip_atomic_load(flg + (F_NONCE + lane * 4 + i) * FW, RLX_AGENT);
            st128(n, wsr, WS_GO + lane * 16, 0);
        };
        if (master) { WAIT_VM(0); echo(); }
        await(sb, C_XN, 8);
        {
            const int uo = WS_UT + ((cx.mtile * 4 + (s >> 1)) * DM + (s & 1) * 288 + (q & 1) * 16 + r16) * 32 + (q >> 1) * 16;
            const u32 a = sb + s * 9 * 1024 + (((r16 >> 1) & 1) * 16 + 4 * q + (r16 >> 2)) * 16 + (r16 & 1) * 8;
#pragma unroll 1
            for (int c = 0; c < 9; ++c) st128(widen(tr_read(a + c * 1024), tr_read(a + c * 1024 + 512)), wsr, uo, c * 1024);
            arrive(sb, C_1A); arrive(sb, C_1B);
        }
        WAIT_VM(0);
#pragma unroll 1
        for (int spin = 0; spin < SPIN_CAP; ++spin) {
            if (master) echo();
            if ((u32)__builtin_amdgcn_readfirstlane(__hip_atomic_load(gow + wg, RLX_AGENT)) == nonce) break;
            __builtin_amdgcn_s_sleep(GNAP);
        }
        if (lane == 0) __hip_atomic_store(flg + F_U + wg * FW, 1u, RLX_AGENT);
        arrive(sb, C_F);
#pragma unroll 1
        for (int spin = 0; spin < SPIN_CAP; ++spin) {
            if (!master) __builtin_amdgcn_s_sleep(UNAP);
            u32 f = 1u;
#pragma unroll
            for (int i = 0; i < 4; ++i) f &= __hip_atomic_load(flg + F_U + (i * 64 + lane) * FW, RLX_AGENT);
            if (__builtin_amdgcn_ballot_w64(f != 1u) == 0) break;
            if (master) echo();
        }
    }
    TL();
    u32 tgt = 0;
    auto sync = [&]() AI { arrive(sb, C_C); tgt += 4; await<2>(sb, C_C, tgt); };
    u32x4 ke[3][2];
    {
        sync();
        if (wb < 3) {
            const u32 kc = sb + L_C + wb * 1344;
            lds_w64(kc + 2 * (128 * (q >> 1) + 4 * (q & 1) + 8 * r16), kc0);
            if (r16 <= 8) lds_w64(kc + 2 * (512 + 16 * r16 + 4 * q), kc2);
            if (q < 2) lds_w64(kc + 2 * (256 + 64 * q + 4 * r16), kc1);
            else {
                lds_w32(kc + 2 * (384 + 64 * (q - 2) + 2 * r16), kc1[0]);
                lds_w32(kc + 2 * (416 + 64 * (q - 2) + 2 * r16), kc1[1]);
            }
        }
        sync();
        const int ci = wb * 64 + lane;
        u32 e[3][9];
#pragma unroll
        for (int j = 0; j < 9; ++j) {
            const int l = 2047 - 8 * ci - j;
            const int code = (int)(pack2((float)l, 0.f) & 0xffffu) - 17024;
            const u32 ia = sb + L_C + 2 * (l < 128 ? (l < 0 ? 0 : l) : code);
#pragma unroll
            for (int c = 0; c < 3; ++c) e[c][j] = *(LAS u16*)(ia + c * 1344);
        }
#pragma unroll
        for (int c = 0; c < 3; ++c) {
            if (ci == 255) e[c][8] = 0;
            ke[c][0] = u32x4{e[c][0] | e[c][1] << 16, e[c][2] | e[c][3] << 16, e[c][4] | e[c][5] << 16, e[c][6] | e[c][7] << 16};
            ke[c][1] = u32x4{e[c][1] | e[c][2] << 16, e[c][3] | e[c][4] << 16, e[c][5] | e[c][6] << 16, e[c][7] | e[c][8] << 16};
            asm volatile("" : "+v"(ke[c][0]), "+v"(ke[c][1]));
        }
    }
    u32x4 uv[3];
#pragma unroll
    for (int c = 0; c < 3; ++c)
        uv[c] = __builtin_amdgcn_raw_buffer_load_b128(wsr, WS_UT + (wb * 32 + (lane >> 1)) * (DM * 32) + (lane & 1) * 16, (c < 2 ? wg * 2 + c : chC) * 32, 16);
    auto put = [&](int c, u32 cv) AI {
        if (wb == c) {
            const u32 zo = lane < 30 ? lane * 16 : lane < 34 ? 4608 + 4096 + (lane - 30) * 16 : 4608 + KR_ODD + 4096 + (lane - 34) * 16;
            if (lane < 38) lds_w128(cv + zo, u32x4{0, 0, 0, 0});
        }
        const u32 o = cv + wb * 1024 + lane * 16;
        lds_w128(o + 480, uv[c]); lds_w128(o + 4608, ke[c][0]); lds_w128(o + 4608 + KR_ODD, ke[c][1]);
    };
    sync();
    put(2, sb + L_C);
    sync();
    TL();
    f32x4 cq[2];
    {
        const u32 cv = sb + L_C;
        if (qt == 0) conv_acc<0x81, 0x80, 2>(cv, wb * 2, lane, cq);
        else if (qt == 1) conv_acc<0x42, 0x40, 2>(cv, wb * 2, lane, cq);
        else if (qt == 2) conv_acc<0x24, 0x20, 2>(cv, wb * 2, lane, cq);
        else conv_acc<0x18, 0x18, 2>(cv, wb * 2, lane, cq);
        sync();
        const u32 xs = cv + lane * 16;
        lds_wf4(xs + wb * 2048, cq[0]); lds_wf4(xs + wb * 2048 + 1024, cq[1]);
        sync();
        if (wb == 0) {
#pragma unroll
            for (int j = 1; j < 4; ++j) { cq[0] += lds_rf4(xs + j * 2048); cq[1] += lds_rf4(xs + j * 2048 + 1024); }
            st128(widen(pack4(cq[0]), pack4(cq[1])), wsr, WS_YC + (chC - 512) * 4096 + ((q & 1) ? 7 - qt : qt) * 512 + r16 * 32 + (q >> 1) * 16, 0);
        }
    }
    Side sd;
    if (wb == 1) sd = load_side(p, cx, 8 + (s & 1));
    TL();
    await<4>(sb, C_G, 4);
    TL();
    __builtin_amdgcn_s_setprio(PRIO_C2);
    put(0, sb + L_CV1);
    put(1, sb + L_CV1 + CV_LDS);
    sync();
    {
        f32x4 ca[4];
        const u32 cv = sb + L_CV1 + (wb >> 1) * CV_LDS;
        if (wb & 1) conv_acc<0x66, 0x60, 8>(cv, 0, lane, ca);
        else conv_acc<0x99, 0x80, 8>(cv, 0, lane, ca);
        TL();
        const u32x2 p0 = pack4(ca[0]), p1 = pack4(ca[1]), p2 = pack4(ca[2]), p3 = pack4(ca[3]);
        const u32 xs = sb + L_C + 8192 + lane * 16;
        const bool up = wb >> 1;
        lds_w128(xs + wb * 1024, up ? u32x4{p0[0], p0[1], p1[0], p1[1]} : u32x4{p2[0], p2[1], p3[0], p3[1]});
        sync();
        const u32x4 o = lds_r128(xs + (wb ^ 2) * 1024);
        const int ymask = ((wb & 1) ? 0x6521 : 0x7430) >> (up ? 8 : 0);
        const int yo = WS_YB + (r16 * 512 + wg * 2 + (q & 1)) * 32 + (q >> 1) * 16;
        st128(up ? widen(u32x2{o[0], o[1]}, p2) : widen(p0, u32x2{o[0], o[1]}), wsr, yo, (ymask & 15) * (16 * 512 * 32));
        st128(up ? widen(u32x2{o[2], o[3]}, p3) : widen(p1, u32x2{o[2], o[3]}), wsr, yo, ((ymask >> 4) & 15) * (16 * 512 * 32));
    }
    WAIT_VM(0);
    TL();
    if (arrive_rtn(sb, C_P + 2) == 3 && lane == 0) __hip_atomic_store(flg + F_Y + wg * FW, 1u, RLX_AGENT);
    if (wb == 1) {
        await<4>(sb, C_P, 4);
        reduce_chunk(p, cx, sd, 8 + (s & 1), F_P, sb + L_C + 12288, true);
    }
    TLEND();
}

__global__ __launch_bounds__(768, 3) void main_kernel(MainArgs p) {
    extern __shared__ __attribute__((aligned(1024))) char dsm[];
    Ctx cx;
    TL();
    const u32 sb = (u32)(size_t)(LAS char*)dsm;
    const int tid = __builtin_amdgcn_workitem_id_x();
    const int lane = tid & 63, w = __builtin_amdgcn_readfirstlane(tid >> 6);
    const int bid = __builtin_amdgcn_workgroup_id_x();
    const int xg = bid & 7, xi = bid >> 3;
    const int s = ((xg & ((8 >> XM) - 1)) << XM) | (xi & ((1 << XM) - 1));
    const int mtile = (xg >> (3 - XM)) * (32 >> XM) + (xi >> XM);
    cx.sb = sb; cx.w = w; cx.xi = xi; cx.s = s; cx.mtile = mtile; cx.wg = mtile * 8 + s;
    if (w >= 8) {
        ssm_body(p, cx);
        __builtin_amdgcn_endpgm();
    }
    const int r16 = lane & 15, q = lane >> 4;
    u32x4 xf[9];
    {
        const u32 xo = (u32)((mtile * 64 + (w >> 1) * 16 + r16) * DM + (w & 1) * 288 + q * 8) * 2;
#pragma unroll
        for (int i = 0; i < 9; ++i) asm volatile("global_load_dwordx4 %0, %1, %2 offset:%3" : "=v"(xf[i]) : "v"(xo), "s"(p.x), "i"(i * 64) : "memory");
    }
    if (w == 0 && lane < 32) lds_w32(sb + L_CNT + lane * 4, 0u);
    WAIT_LGKM0();
    __builtin_amdgcn_s_barrier();
    CFENCE();
    TL();
    const u32 xo = sb + w * 9216;
    {
        const u32x4 ones = {0x3f803f80u, 0x3f803f80u, 0x3f803f80u, 0x3f803f80u};
        f32x4 s1 = {0.f, 0.f, 0.f, 0.f}, s2 = {0.f, 0.f, 0.f, 0.f};
        asm volatile("s_waitcnt vmcnt(0)" : "+v"(xf[0]), "+v"(xf[1]), "+v"(xf[2]), "+v"(xf[3]), "+v"(xf[4]), "+v"(xf[5]), "+v"(xf[6]), "+v"(xf[7]), "+v"(xf[8]) :: "memory");
        TL();
#pragma unroll
        for (int i = 0; i < 9; ++i) { s1 = mfma16(ones, xf[i], s1); s2 = mfma16(xf[i], xf[i], s2); }
        MFMA_SETTLE();
        const int r = r16 & 3;
        const f32x2 st = {s1[0], r == 0 ? s2[0] : r == 1 ? s2[1] : r == 2 ? s2[2] : s2[3]};
        if (q == (r16 >> 2)) {
            *(LAS f32x2*)(xo + r16 * 16) = st;
            *(LAS f32x2*)(sb + (w ^ 1) * 9216 + r16 * 16 + 8) = st;
        }
    }
    arrive(sb, C_S + (w >> 1));
    await(sb, C_S + (w >> 1), 2);
    {
        const f32x4 st = lds_rf4(xo + r16 * 16);
        const float m = (st[0] + st[2]) * (1.f / DM);
        const float var = rbf(__builtin_fmaxf((st[1] + st[3]) * (1.f / DM) - m * m, 0.f));
        const float rs = __builtin_amdgcn_rcpf(rbf(__builtin_amdgcn_sqrtf(rbf(var + 1e-5f)))), sh = -rbf(m) * rs;
#pragma unroll
        for (int i = 0; i < 9; ++i) {
            u32x4 o;
#pragma unroll
            for (int j = 0; j < 4; ++j) o[j] = pack2(__builtin_fmaf(bf_lo(xf[i][j]), rs, sh), __builtin_fmaf(bf_hi(xf[i][j]), rs, sh));
            lds_w128(xo + i * 1024 + lane * 16, o);
        }
    }
    arrive(sb, C_XN);
    TL();
    if (w < 4) { mlp_body<5>(p, cx); __builtin_amdgcn_endpgm(); }
    mlp_body<4>(p, cx);
}
#if defined(__HIP_DEVICE_COMPILE__)
#pragma clang attribute pop
#endif

extern "C" void kernel_launch(void* const* d_in, const int* in_sizes, int n_in, void* d_out, int out_size,
                              void* d_ws, size_t ws_size, hipStream_t stream) {
    (void)hipFuncSetAttribute((const void*)main_kernel, hipFuncAttributeMaxDynamicSharedMemorySize, MAIN_LDS);
    MainArgs a{(const u16*)d_in[0], (const u16*)d_in[1], (const u16*)d_in[2], (const u16*)d_in[3], (const u16*)d_in[4],
               (const u16*)d_in[5], (const u16*)d_in[6], (const u32*)d_in[7], (const u32*)d_in[8], (const char*)d_in[10],
               (const char*)d_in[11], (const char*)d_in[12], (const u16*)d_in[13], (char*)d_ws, (u16*)d_out};
    main_kernel<<<256, 768, MAIN_LDS, stream>>>(a);
}
```

```cpp
#include <hip/hip_runtime.h>
#include <stdint.h>
#include <type_traits>

typedef unsigned short u16;
typedef unsigned int u32;
typedef __attribute__((ext_vector_type(2))) __bf16 bf16x2;
typedef __attribute__((ext_vector_type(8))) __bf16 bf16x8;
typedef __attribute__((ext_vector_type(4))) short s16x4;
typedef __attribute__((ext_vector_type(2))) float f32x2;
typedef __attribute__((ext_vector_type(4))) float f32x4;
typedef __attribute__((ext_vector_type(2))) unsigned u32x2;
typedef __attribute__((ext_vector_type(4))) unsigned u32x4;
#define LAS __attribute__((address_space(3)))
#define GAS __attribute__((address_space(1)))
#define CAS __attribute__((address_space(4)))
#define DI __device__ __forceinline__
#define AI __attribute__((always_inline))
#if defined(__HIP_DEVICE_COMPILE__)
#pragma clang attribute push(__attribute__((target("no-packed-fp32-ops"))), apply_to = function)
#endif
#define SB() __builtin_amdgcn_sched_barrier(0)
#define CFENCE() asm volatile("" ::: "memory")
#define MFMA_SETTLE() do { SB(); asm volatile("s_nop 15\n\ts_nop 15"); SB(); } while (0)
#define WAIT_VM(n) asm volatile("s_waitcnt vmcnt(%0)" :: "i"(n) : "memory")
#define WAIT_LGKM0() do { CFENCE(); __builtin_amdgcn_s_waitcnt(0xC07F); CFENCE(); } while (0)
#define RLX_AGENT __ATOMIC_RELAXED, __HIP_MEMORY_SCOPE_AGENT
#define RLX_WG __ATOMIC_RELAXED, __HIP_MEMORY_SCOPE_WORKGROUP

constexpr int SEQ = 2048, DM = 576, NST = 128;
constexpr int FLAG_STRIDE = 128, FW = FLAG_STRIDE / 4;
constexpr int XM = 2;
constexpr unsigned CODE_BYTES = 47104;
constexpr int WS_FLAG = 0;
constexpr int WS_NONCE = 256 * FLAG_STRIDE, WS_U = WS_NONCE + 1024, WS_GO = WS_U + 1024;
constexpr int WS_UT = WS_GO + 1024;
constexpr int WS_YB = WS_UT + SEQ * DM * 2;
constexpr int WS_YC = WS_YB + SEQ * 512 * 2;
constexpr int WS_PART = WS_YB + SEQ * DM * 2;

constexpr int A_BYTES = 73728;
constexpr int L_RINGA = A_BYTES, L_RINGB = L_RINGA + 40960, L_C = L_RINGB + 32768;
constexpr int KR_ODD = 4160, CV_LDS = 4608 + 8320;
constexpr int KG_ROW = 272, KG_IMG = 4608;
constexpr int L_AX = L_RINGA, L_CV1 = L_RINGA + 12288;
constexpr int L_BIAS = 161792, L_CNT = L_BIAS + 1152, L_SINK = L_CNT + 128;
constexpr int MAIN_LDS = 163840;
constexpr int SPIN_CAP = 1 << 20;
constexpr u32 NOTYET = 0xffffffffu;
enum { C_S = 0, C_XN = 4, C_W, C_1A, C_1B, C_HA, C_HB, C_F, C_C, C_E, C_G, C_B2, C_OK, C_P = C_OK + 2 };

DI float bf_lo(u32 v) { return __builtin_bit_cast(float, v << 16); }
DI float bf_hi(u32 v) { return __builtin_bit_cast(float, v & 0xffff0000u); }
DI u32 pack2(float lo, float hi) {
    f32x2 v = {lo, hi};
    return __builtin_bit_cast(u32, __builtin_convertvector(v, bf16x2));
}
DI u32x2 pack4(f32x4 c) { return u32x2{pack2(c[0], c[1]), pack2(c[2], c[3])}; }
DI float rbf(float f) { return bf_lo(pack2(f, f)); }
DI f32x4 mfma16(u32x4 a, u32x4 b, f32x4 c) {
    return __builtin_amdgcn_mfma_f32_16x16x32_bf16(__builtin_bit_cast(bf16x8, a), __builtin_bit_cast(bf16x8, b), c, 0, 0, 0);
}
DI void mfma16_acc(f32x4& c, u32x4 a, u32x4 b) {
    asm volatile("v_mfma_f32_16x16x32_bf16 %0, %1, %2, %0" : "+v"(c) : "v"(a), "v"(b));
}
DI void mfma16_set(f32x4& d, u32x4 a, u32x4 b, f32x4 c) {
    asm volatile("v_mfma_f32_16x16x32_bf16 %0, %1, %2, %3" : "=&v"(d) : "v"(a), "v"(b), "v"(c));
}
DI void mfma16_zero(f32x4& d, u32x4 a, u32x4 b) {
    asm volatile("v_mfma_f32_16x16x32_bf16 %0, %1, %2, 0" : "=&v"(d) : "v"(a), "v"(b));
}
DI u32x2 tr_read(u32 addr) {
    return __builtin_bit_cast(u32x2, __builtin_amdgcn_ds_read_tr16_b64_v4i16((LAS s16x4*)addr));
}
DI u32x4 lds_r128(u32 addr) { return *(LAS u32x4*)addr; }
DI void lds_w128(u32 addr, u32x4 v) { *(LAS u32x4*)addr = v; }
DI void lds_w64(u32 addr, u32x2 v) { *(LAS u32x2*)addr = v; }
DI void lds_w32(u32 addr, u32 v) { *(LAS u32*)addr = v; }
DI f32x4 lds_rf4(u32 addr) { return *(LAS f32x4*)addr; }
DI void lds_wf4(u32 addr, f32x4 v) { *(LAS f32x4*)addr = v; }
DI int fresh_lane() {
    int l;
    asm volatile("v_mbcnt_lo_u32_b32 %0, -1, 0\n\tv_mbcnt_hi_u32_b32 %0, -1, %0" : "=v"(l));
    return l;
}
DI void glds16(const char* sbase, u32 voff, u32 lds) {
    asm volatile("s_mov_b32 m0, %2\n\ts_nop 0\n\tglobal_load_lds_dwordx4 %0, %1" :: "v"(voff), "s"(sbase), "s"(lds) : "memory");
}
template <int OFF>
DI void glds16_next(const char* sbase, u32 voff) {
    asm volatile("global_load_lds_dwordx4 %0, %1 offset:%2" :: "v"(voff), "s"(sbase), "i"(OFF) : "memory");
}
DI void touch(const char* sbase, u32 voff, u32 lds) {
    asm volatile("s_mov_b32 m0, %2\n\ts_nop 0\n\tglobal_load_lds_dword %0, %1" :: "v"(voff), "s"(sbase), "s"(lds) : "memory");
}
DI void sprefetch(const char* b, int g0, int dg) {
#pragma unroll 1
    for (int g = g0; g < 648; g += dg) {
        const char* a = b + g * 1024;
        u32 t0;
        asm volatile("s_load_dword %0, %1, 0x0\n\ts_load_dword %0, %1, 0x40\n\ts_load_dword %0, %1, 0x80\n\ts_load_dword %0, %1, 0xc0\n\t"
                     "s_load_dword %0, %1, 0x100\n\ts_load_dword %0, %1, 0x140\n\ts_load_dword %0, %1, 0x180\n\ts_load_dword %0, %1, 0x1c0\n\t"
                     "s_load_dword %0, %1, 0x200\n\ts_load_dword %0, %1, 0x240\n\ts_load_dword %0, %1, 0x280\n\ts_load_dword %0, %1, 0x2c0\n\t"
                     "s_load_dword %0, %1, 0x300\n\ts_load_dword %0, %1, 0x340\n\ts_load_dword %0, %1, 0x380\n\ts_load_dword %0, %1, 0x3c0\n\t"
                     "s_waitcnt lgkmcnt(0)" : "=&s"(t0) : "s"(a) : "memory");
    }
}
template <class R>
DI void st128(u32x4 v, R rsrc, int voff, int soff) {
    __builtin_amdgcn_raw_buffer_store_b128(v, rsrc, voff, soff, 16);
    SB(); asm volatile("s_nop 1"); SB();
}
DI u32x4 widen(u32x2 a, u32x2 b) {
    const auto r0 = __builtin_amdgcn_permlane16_swap(a[0], b[0], false, false);
    const auto r1 = __builtin_amdgcn_permlane16_swap(a[1], b[1], false, false);
    return u32x4{r0[0], r1[0], r0[1], r1[1]};
}
DI u32x4 published(u32x4 v) {
    return u32x4{v[0] < NOTYET ? v[0] : NOTYET - 1, v[1] < NOTYET ? v[1] : NOTYET - 1, v[2] < NOTYET ? v[2] : NOTYET - 1, v[3] < NOTYET ? v[3] : NOTYET - 1};
}
DI float gelu(float v) {
    const float c0 = -2.f * 0.7978845608028654f * 1.4426950408889634f, c1 = c0 * 0.044715f;
    const float e = __builtin_amdgcn_exp2f(v * __builtin_fmaf(v * v, c1, c0));
    return v * __builtin_amdgcn_rcpf(1.f + e);
}
struct cpx { float re, im; };
DI cpx cmul(cpx a, cpx b) { return cpx{a.re * b.re - a.im * b.im, a.re * b.im + a.im * b.re}; }

DI void arrive(u32 sb, int c) {
    unsigned long long ex;
    asm volatile("s_mov_b64 %0, exec\n\ts_mov_b64 exec, 1\n\tds_add_u32 %1, %2\n\ts_mov_b64 exec, %0"
                 : "=&s"(ex) : "v"(sb + L_CNT + c * 4), "v"(1u) : "memory");
}
DI u32 arrive_rtn(u32 sb, int c) {
    unsigned long long ex;
    u32 r;
    asm volatile("s_mov_b64 %0, exec\n\ts_mov_b64 exec, 1\n\tds_add_rtn_u32 %1, %2, %3\n\ts_mov_b64 exec, %0\n\ts_waitcnt lgkmcnt(0)"
                 : "=&s"(ex), "=&v"(r) : "v"(sb + L_CNT + c * 4), "v"(1u) : "memory");
    return (u32)__builtin_amdgcn_readfirstlane(r);
}
template <int NAP = 1>
DI void await(u32 sb, int c, u32 n) {
    CFENCE();
#pragma unroll 1
    for (int i = 0; i < SPIN_CAP; ++i) {
        if ((u32)__builtin_amdgcn_readfirstlane(__hip_atomic_load((LAS u32*)(sb + L_CNT + c * 4), RLX_WG)) >= n) break;
        __builtin_amdgcn_s_sleep(NAP);
    }
    CFENCE();
}
DI void poll(GAS u32* f) {
#pragma unroll 1
    for (int spin = 0; spin < SPIN_CAP; ++spin) {
        if (__builtin_amdgcn_ballot_w64(__hip_atomic_load(f, RLX_AGENT) != 1u) == 0) break;
        __builtin_amdgcn_s_sleep(2);
    }
    CFENCE();
}

struct MainArgs {
    const u16 *x, *Are, *Aim, *Bre, *Bim, *Cre, *Cim;
    const u32 *logdt, *dskip;
    const char *w1, *b1, *w2;
    const u16* b2;
    char* ws;
    u16* out;
};
struct Ctx {
    u32 sb;
    int w, s, mtile, wg;
};

template <bool W1>
DI void stage_offsets(u32* vo, int lane, int w) {
    constexpr int RS = W1 ? 9216 : 1152;
    const int prow = lane >> 3, pch = (lane & 7) ^ (((lane >> 4) & 3) << 1), xr = lane >> 1;
    vo[0] = (u32)((W1 ? 8 * (prow >> 2) + (prow & 3) : prow) * RS + pch * 16);
    vo[1] = vo[0] + (W1 ? 16 : 8) * RS - 1024; vo[2] = vo[0] + (W1 ? 4 : 16) * RS - 2048; vo[3] = vo[0] + (W1 ? 20 : 24) * RS - 3072;
    vo[4] = (u32)((W1 ? 8 * ((xr >> 2) & 3) + 4 * (xr >> 4) + (xr & 3) : xr) * RS + 512 - w * 96 + (lane & 1) * 16);
}
template <int NT>
DI void stage_piece(const char*& gp, const u32* vo, u32 ring, int adv, int slot, int i) {
    if (i == 0) glds16(gp, vo[0], ring + slot * NT * 1024);
    if (i == 1) glds16_next<1024>(gp, vo[1]);
    if (i == 2) glds16_next<2048>(gp, vo[2]);
    if (i == 3) glds16_next<3072>(gp, vo[3]);
    if (i == 4) glds16(gp, vo[4], ring + slot * NT * 1024 + 4096);
    if (i == NT - 1) gp += adv;
}

struct Side { u32x2 xa, xb, ba, bb; };
DI Side load_side(const MainArgs& p, Ctx& cx, int pr) {
    const int lane = fresh_lane(), d0 = pr * 32 + (lane >> 4) * 4;
    const u16* xp = p.x + (size_t)((cx.mtile * 4 + (cx.s >> 1)) * 16 + (lane & 15)) * DM + d0;
    return Side{*(const u32x2*)xp, *(const u32x2*)(xp + 16), *(const u32x2*)(p.b2 + d0), *(const u32x2*)(p.b2 + d0 + 16)};
}
DI void reduce_chunk(const MainArgs& p, Ctx& cx, const Side& sd, int pr, u32 own, u32 ys) {
    const int lane = fresh_lane(), r16 = lane & 15, q = lane >> 4;
    const int tt = cx.mtile * 4 + (cx.s >> 1);
    const auto wsr = __builtin_amdgcn_make_buffer_rsrc((void*)p.ws, 0, 0x7fffffff, 0x00020000);
    const size_t ro = (size_t)(tt * 16 + r16) * DM;
    const u32x2 xa = sd.xa, xb = sd.xb, ba = sd.ba, bb = sd.bb;
    const int yo = pr < 16 ? WS_YB + (tt * 512 + pr * 32) * 32 + lane * 16 : WS_YC + ((pr - 16) * 32 + (lane >> 1)) * 4096 + tt * 32 + (lane & 1) * 16;
    const int pi = WS_PART + cx.mtile * 8 * A_BYTES + ((cx.s >> 1) * 18 + pr) * 1024 + lane * 16;
    u32x4 pt[8];
#pragma unroll
    for (int i = 0; i < 7; ++i) pt[i] = __builtin_amdgcn_raw_buffer_load_b128(wsr, pi, ((cx.s + 1 + i) & 7) * A_BYTES, 16);
    pt[7] = lds_r128(own + lane * 16);
    u32x4 yv = __builtin_amdgcn_raw_buffer_load_b128(wsr, yo, 0, 16);
    CFENCE();
    float o[8];
#pragma unroll
    for (int i = 0; i < 8; ++i) {
        const int j = (i >> 1) & 1;
        const u32 xw = i < 4 ? xa[j] : xb[j], bw = i < 4 ? ba[j] : bb[j];
        o[i] = (i & 1) ? bf_hi(xw) + bf_hi(bw) : bf_lo(xw) + bf_lo(bw);
    }
#pragma unroll
    for (int k = 0; k < 8; ++k)
#pragma unroll
        for (int i = 0; i < 8; ++i) o[i] += (i & 1) ? bf_hi(pt[k][i >> 1]) : bf_lo(pt[k][i >> 1]);
#pragma unroll
    for (int i = 0; i < 8; ++i) asm volatile("" : "+v"(o[i]));
#pragma unroll 1
    for (int spin = 0; spin < SPIN_CAP; ++spin) {
        if (__builtin_amdgcn_ballot_w64(yv[0] == NOTYET || yv[1] == NOTYET || yv[2] == NOTYET || yv[3] == NOTYET) == 0) break;
        __builtin_amdgcn_s_sleep(2);
        CFENCE();
        yv = __builtin_amdgcn_raw_buffer_load_b128(wsr, yo, 0, 16);
    }
    lds_w128(ys + lane * 16, yv);
    const u32 ta = ys + (4 * q + (r16 >> 2)) * 32 + 8 * (r16 & 3);
    const u32x2 y0 = tr_read(ta), y1 = tr_read(ta + 512);
#pragma unroll
    for (int i = 0; i < 8; ++i) {
        const u32 yz = i < 4 ? y0[(i >> 1) & 1] : y1[(i >> 1) & 1];
        o[i] += (i & 1) ? bf_hi(yz) : bf_lo(yz);
    }
    const auto osr = __builtin_amdgcn_make_buffer_rsrc((void*)p.out, 0, SEQ * DM * 2, 0x00020000);
    __builtin_amdgcn_raw_buffer_store_b128(widen(u32x2{pack2(o[0], o[1]), pack2(o[2], o[3])}, u32x2{pack2(o[4], o[5]), pack2(o[6], o[7])}),
                                           osr, ((int)ro + pr * 32 + (q & 1) * 16 + (q >> 1) * 8) * 2, 0, 0);
}

template <int NT>
DI void mlp_body(const MainArgs& p, Ctx& cx) {
    constexpr bool GA = NT == 5;
    constexpr int SLOT = NT * 1024, GI = GA ? 0 : 1;
    const u32 sb = cx.sb;
    const int w = cx.w, s = cx.s, wb = w & 3, g = GA ? w : w + 1;
    const u32 ring = sb + (GA ? L_RINGA + w * 10240 : L_RINGB + wb * 8192);
    const char* gp = p.w1 + s * 1152 + g * 128 + 64 * 9216;
    int adv = 32 * 9216;
    u32 vo[5], ta, tx, a0;
    f32x4 acc[NT][4], bias[NT];
    {
        const int lane = fresh_lane(), r16 = lane & 15, q = lane >> 4, trow = 4 * q + (r16 >> 2);
        stage_offsets<true>(vo, lane, w);
        ta = ring + trow * 128 + 32 * ((trow >> 1) & 3) + 8 * (r16 & 3);
        tx = ring + 4096 + trow * 32 + 8 * (r16 & 3);
        a0 = sb + lane * 16;
        await(sb, C_W, 1);
        u32x2 bv[NT];
#pragma unroll
        for (int t = 0; t < NT; ++t) bv[t] = *(LAS u32x2*)(sb + L_BIAS + q * 8 + (t < 4 ? g * 128 + t * 32 : 512 + w * 32));
#pragma unroll
        for (int t = 0; t < NT; ++t) bias[t] = f32x4{bf_lo(bv[t][0]), bf_hi(bv[t][0]), bf_lo(bv[t][1]), bf_hi(bv[t][1])};
#pragma unroll
        for (int t = 0; t < NT; ++t) asm volatile("" : "+v"(bias[t]));
        await(sb, C_XN, 8);
    }
    auto piece = [&](int slot, int i) AI { stage_piece<NT>(gp, vo, ring, adv, slot, i); };
    const int pr = (GA ? 0 : 10) + (s & 1) * 4 + wb;
    Side sd;
#pragma unroll
    for (int ph = 0; ph < 2; ++ph) {
        u32 aa = a0;
        u32x4 fw[2][NT], fx[4];
        auto rdw = [&](int b, int t) AI {
            const u32 a = t < 4 ? (ta ^ (t << 5)) + b * SLOT : tx + b * SLOT;
            const u32x2 lo = tr_read(a), hi = tr_read(a + (t < 4 ? 2048 : 512));
            fw[b][t] = u32x4{lo[0], lo[1], hi[0], hi[1]};
        };
        auto rdx = [&](int m) AI { fx[m] = lds_r128(aa + m * 18432); if (m == 3) aa += 1024; };
        auto mf = [&](int b, int i, bool first) AI {
            SB();
            if (!first) mfma16_acc(acc[i % NT][i / NT], fw[b][i % NT], fx[i / NT]);
            else if (ph == 0) mfma16_set(acc[i % NT][i / NT], fw[b][i % NT], fx[i / NT], bias[i % NT]);
            else mfma16_zero(acc[i % NT][i / NT], fw[b][i % NT], fx[i / NT]);
            SB();
        };
        auto rdh = [&](int b, int t, int h) AI {
            const u32x2 v = tr_read((ta ^ (t << 5)) + b * SLOT + h * 2048);
            fw[b][t][2 * h] = v[0]; fw[b][t][2 * h + 1] = v[1];
        };
        auto step = [&](int b, bool st, bool first, auto v0, auto v1) AI {
            constexpr int M = 2 * NT;
            WAIT_VM(decltype(v0)::value);
#pragma unroll
            for (int i = 0; i < 4 * NT; ++i) {
                mf(b, i, first);
                if (i < 2) { rdh(b ^ 1, 2 * i, 0); rdh(b ^ 1, 2 * i + 1, 0); }
                if (i == M - 1) WAIT_VM(decltype(v1)::value);
                if (i >= M && i < M + 2) { rdh(b ^ 1, 2 * (i - M), 1); rdh(b ^ 1, 2 * (i - M) + 1, 1); }
                if (GA && i == M + 2) rdw(b ^ 1, 4);
                if (i % NT == NT - 1) rdx(i / NT);
                if (i == NT || i == 3 * NT) { CFENCE(); __builtin_amdgcn_s_waitcnt(0xC17F); CFENCE(); }
                if (st) {
                    if (i == NT + 1 || i == NT + 2) piece(b ^ 1, i - NT - 1);
                    if (i > 3 * NT && i <= 3 * NT + NT - 2) piece(b ^ 1, i - 3 * NT + 1);
                }
            }
        };
        using std::integral_constant;
        const integral_constant<int, 2 * NT - 2> S0; const integral_constant<int, NT + 2> S1;
        const integral_constant<int, NT> SN; const integral_constant<int, NT - 2> SM; const integral_constant<int, 0> SZ;
        WAIT_VM(NT);
#pragma unroll
        for (int t = 0; t < NT; ++t) rdw(0, t);
#pragma unroll
        for (int m = 0; m < 4; ++m) rdx(m);
        WAIT_LGKM0();
#pragma unroll
        for (int i = 0; i < NT; ++i) piece(0, i);
        step(0, true, true, S0, S1);
#pragma unroll 1
        for (int it = 0; it < 7; ++it) {
            if (it == 4) {
                if (ph == 0) { arrive(sb, C_1A); WAIT_LGKM0(); }
                else await(sb, C_HB, 4);
            }
            step(1, true, false, S0, S1);
            step(0, true, false, S0, S1);
        }
        if (ph == 0) {
            gp = p.w2 + (size_t)s * 576 * 1152 + g * 128;
            stage_offsets<false>(vo, fresh_lane(), w);
            adv = 32 * 1152;
            step(1, true, false, S0, S1);
            step(0, true, false, S0, S1);
        } else {
            step(1, false, false, S0, SN);
            step(0, false, false, SM, SZ);
            if (!GA) { sd = load_side(p, cx, pr); CFENCE(); }
        }
        WAIT_LGKM0();
        if (ph == 0) arrive(sb, C_1B);
        else arrive(sb, GA ? C_G : C_B2);
#pragma unroll
        for (int i = 0; i < 4 * NT; ++i) mf(1, i, false);
        MFMA_SETTLE();
        if (ph == 1) break;
        u32x2 hp[NT][4];
#pragma unroll
        for (int t = 0; t < NT; ++t)
#pragma unroll
            for (int m = 0; m < 4; ++m) {
                hp[t][m][0] = pack2(gelu(acc[t][m][0]), gelu(acc[t][m][1]));
                hp[t][m][1] = pack2(gelu(acc[t][m][2]), gelu(acc[t][m][3]));
            }
#pragma unroll
        for (int t = 0; t < NT; ++t) asm volatile("" : "+v"(hp[t][0]), "+v"(hp[t][1]), "+v"(hp[t][2]), "+v"(hp[t][3]));
        await(sb, GA ? C_1A : C_1B, 9);
#pragma unroll
        for (int m = 0; m < 4; ++m) {
            lds_w128(a0 + (m * 18 + 2 * g) * 1024, u32x4{hp[0][m][0], hp[0][m][1], hp[1][m][0], hp[1][m][1]});
            lds_w128(a0 + (m * 18 + 2 * g + 1) * 1024, u32x4{hp[2][m][0], hp[2][m][1], hp[3][m][0], hp[3][m][1]});
            if (GA) lds_w64(a0 + (m * 18 + 8 + (w >> 1)) * 1024 + (w & 1) * 8, hp[NT - 1][m]);
        }
        arrive(sb, GA ? C_HA : C_HB);
        await(sb, C_HA, 4);
    }
    if (GA) {
        __builtin_amdgcn_s_setprio(0);
        sd = load_side(p, cx, pr);
        CFENCE();
    }
    bool last;
    {
        const int lane = fresh_lane();
        const auto wsr = __builtin_amdgcn_make_buffer_rsrc((void*)p.ws, 0, 0x7fffffff, 0x00020000);
        const int po = WS_PART + cx.wg * A_BYTES + lane * 16;
        const int mo = (wb >> 1) == (s & 1) ? s >> 1 : -1;
        if (GA) {
            await(sb, C_G, 4);
#pragma unroll
            for (int k = 0; k < 4; ++k) lds_w64(sb + L_AX + ((w >> 1) * 4 + k) * 1024 + lane * 16 + (w & 1) * 8, pack4(acc[NT - 1][k]));
            arrive(sb, C_E);
        }
#pragma unroll
        for (int i = 0; i < 8; ++i) {
            const int m = i >> 1, h = i & 1;
            const u32x2 lo = pack4(acc[2 * h][m]), hi = pack4(acc[2 * h + 1][m]);
            const u32x4 pv = {lo[0], lo[1], hi[0], hi[1]};
            if (m == mo) lds_w128((GA ? sb + L_AX + 8192 + (wb & 1) * 2048 : ring + 4096) + h * 1024 + lane * 16, pv);
            else {
                st128(pv, wsr, po, (m * 18 + 2 * g + h) * 1024);
                if (GA) { __builtin_amdgcn_s_sleep(4); SB(); }
            }
        }
        if (GA) {
            await(sb, C_E, 4);
#pragma unroll
            for (int i = 0; i < 2; ++i)
                if ((w & 1) * 2 + i != mo)
                    st128(lds_r128(sb + L_AX + (w * 2 + i) * 1024 + lane * 16), wsr, po, (((w & 1) * 2 + i) * 18 + 8 + (w >> 1)) * 1024);
        }
        await(sb, C_F, 1);
        WAIT_VM(0);
        last = arrive_rtn(sb, C_P + GI) == 3;
        if (last && lane == 0) __hip_atomic_store((GAS u32*)(p.ws + WS_FLAG) + GI + cx.wg * FW, 1u, RLX_AGENT);
    }
    __builtin_amdgcn_s_setprio(0);
    await<4>(sb, GA ? C_B2 : C_G, 4);
    if (last) {
        poll((GAS u32*)(p.ws + WS_FLAG) + GI + (cx.mtile * 8 + (fresh_lane() & 7)) * FW);
        arrive(sb, C_OK + GI);
    } else await(sb, C_OK + GI, 1);
    reduce_chunk(p, cx, sd, pr, GA ? sb + L_AX + 8192 + w * 1024 : sb + L_RINGB + ((s & 1) * 2 + (wb >> 1)) * 8192 + 4096 + (wb & 1) * 1024,
                 GA ? sb + L_AX + 8192 + w * 1024 : sb + L_RINGB + wb * 1024);
}

constexpr int hi_bit(int m) { int r = 0; for (int i = 0; i < 8; ++i) if (m >> i & 1) r = i; return r; }
constexpr int pop_below(int m, int b) { int r = 0; for (int i = 0; i < b; ++i) r += m >> i & 1; return r; }
struct ConvSched { int n; int acc[36]; int nt[36]; int a[36]; };
constexpr ConvSched conv_sched(int mask, int split) {
    ConvSched s{};
    int rem[16] = {}, nts[16] = {}, nxt[8] = {}, last[16] = {}, na = 0;
    for (int t = 0; t < 8; ++t) if (mask >> t & 1) {
        const int parts = (split >> t & 1) ? 2 : 1;
        for (int k = 0; k < parts; ++k) { nts[na] = t; rem[na] = (t + 1 + (parts - 1 - k)) / parts; last[na] = -100; ++na; }
        s.n += t + 1;
    }
    for (int i = 0; i < s.n; ++i) {
        int best = -1;
        for (int gap = 4; gap >= 1 && best < 0; --gap)
            for (int k = 0; k < na; ++k)
                if (rem[k] > 0 && i - last[k] >= gap && (best < 0 || rem[k] > rem[best])) best = k;
        s.acc[i] = best; s.nt[i] = nts[best]; s.a[i] = nxt[nts[best]]++;
        --rem[best]; last[best] = i;
    }
    return s;
}
template <int MASK, int SPLIT, int NB>
DI void conv_acc(u32 ub, int b0, int lane, f32x4* out) {
    constexpr int H = hi_bit(MASK) + 1, NA = pop_below(MASK, 8) + pop_below(SPLIT, 8);
    constexpr ConvSched S = conv_sched(MASK, SPLIT);
    const int r16 = lane & 15, q = lane >> 4;
    const u32 k0 = ub + 4608 + ((r16 & 1) ? (2031 + 8 * q - r16) * 2 : KR_ODD + (2030 + 8 * q - r16) * 2) - 64 * b0;
    u32 ka[4], ua = ub + 448 + r16 * 32 + q * 16 - 64 * b0;
#pragma unroll
    for (int i = 0; i < 4; ++i) ka[i] = k0 - 512 - 1024 * i;
    f32x4 acc[NA];
#pragma unroll
    for (int i = 0; i < NA; ++i) { acc[i] = f32x4{0.f, 0.f, 0.f, 0.f}; asm volatile("" : "+v"(acc[i])); }
    SB();
#pragma unroll 1
    for (int b = 0; b < NB; ++b) {
        u32x4 fa[H], fb[H];
#pragma unroll
        for (int a = 0; a < H; ++a) {
            const LAS u32* kp = (const LAS u32*)(ka[a >> 1]) + ((a & 1) ? 0 : 128);
            fa[a] = u32x4{kp[0], kp[1], kp[2], kp[3]};
            fb[H - 1 - a] = lds_r128(ua + 512 * (H - 1 - a));
        }
#pragma unroll
        for (int i = 0; i < (H + 1) / 2; ++i) { ka[i] -= 64; asm volatile("" : "+v"(ka[i])); }
        ua -= 64; asm volatile("" : "+v"(ua));
        SB();
#pragma unroll
        for (int i = 0; i < S.n; ++i) mfma16_acc(acc[S.acc[i]], fa[S.a[i]], fb[S.nt[i] - S.a[i]]);
        SB();
    }
    MFMA_SETTLE();
    int k = 0;
#pragma unroll
    for (int t = 0; t < 8; ++t)
        if (MASK >> t & 1) {
            out[pop_below(MASK, t)] = (SPLIT >> t & 1) ? acc[k] + acc[k + 1] : acc[k];
            k += (SPLIT >> t & 1) ? 2 : 1;
        }
}

DI void conv_full(u32 ub, int b0, int lane, f32x4* acc) {
    const int r16 = lane & 15, q = lane >> 4;
    const u32 k0 = ub + 4608 + ((r16 & 1) ? (2031 + 8 * q - r16) * 2 : KR_ODD + (2030 + 8 * q - r16) * 2) - 64 * b0;
    u32 ka[4], ua = ub + 448 + r16 * 32 + q * 16 - 64 * b0;
#pragma unroll
    for (int i = 0; i < 4; ++i) ka[i] = k0 - 512 - 1024 * i;
    f32x4 alt[3];
#pragma unroll
    for (int i = 0; i < 8; ++i) { acc[i] = f32x4{0.f, 0.f, 0.f, 0.f}; asm volatile("" : "+v"(acc[i])); }
#pragma unroll
    for (int i = 0; i < 3; ++i) { alt[i] = f32x4{0.f, 0.f, 0.f, 0.f}; asm volatile("" : "+v"(alt[i])); }
    SB();
    u32x4 fa[8], fb[8];
    auto ld = [&](int a) AI {
        const LAS u32* kp = (const LAS u32*)(ka[a >> 1]) + ((a & 1) ? 0 : 128);
        fa[a] = u32x4{kp[0], kp[1], kp[2], kp[3]};
        fb[7 - a] = lds_r128(ua + 512 * (7 - a));
    };
#pragma unroll
    for (int a = 0; a < 8; ++a) ld(a);
#pragma unroll 1
    for (int b = 0; b < 4; ++b) {
        const u32 d = b < 3 ? 64 : 0;
#pragma unroll
        for (int i = 0; i < 4; ++i) { ka[i] -= d; asm volatile("" : "+v"(ka[i])); }
        ua -= d; asm volatile("" : "+v"(ua));
#pragma unroll
        for (int a = 0; a < 8; ++a) {
            SB();
#pragma unroll
            for (int nt = 7; nt >= a; --nt) mfma16_acc((a == 5 || a == 7) ? alt[nt - 5] : acc[nt], fa[a], fb[nt - a]);
            SB();
            ld(a);
        }
    }
    MFMA_SETTLE();
#pragma unroll
    for (int i = 0; i < 3; ++i) acc[5 + i] += alt[i];
}

DI void ssm_body(const MainArgs& p, Ctx& cx) {
    const u32 sb = cx.sb;
    const int wb = cx.w & 3, s = cx.s, wg = cx.wg;
    const int lane = fresh_lane(), r16 = lane & 15, q = lane >> 4;
    const int chC = 512 + (wg >> 2), qt = wg & 3;
    const auto wsr = __builtin_amdgcn_make_buffer_rsrc((void*)p.ws, 0, 0x7fffffff, 0x00020000);
    GAS u32* const gow = (GAS u32*)(p.ws + WS_GO);
    const int ch = wb < 2 ? wg * 2 + wb : chC;

    u32x4 ke[4][2];
    if (wb < 3) {
        const u32 ldw = ((const CAS u32*)p.logdt)[ch >> 1], dsw = ((const CAS u32*)p.dskip)[ch >> 1];
        WAIT_VM(0);
        u32 prm[6];
#pragma unroll
        for (int i = 0; i < 6; ++i) prm[i] = *(LAS u32*)(sb + L_C + wb * KG_IMG + i * 256 + lane * 4);
        CFENCE();
        const float dt = rbf(__builtin_expf((ch & 1) ? bf_hi(ldw) : bf_lo(ldw)));
        const u32 img = sb + L_C + wb * KG_IMG;
        f32x4 k0 = {0.f, 0.f, 0.f, 0.f}, k1 = k0, k2 = k0;
        if (lane == 0) k0[0] = (ch & 1) ? bf_hi(dsw) : bf_lo(dsw);
        const u32 wa = img + lane * 4, ra = img + r16 * KG_ROW + q * 16;
#pragma unroll 1
        for (int hf = 0; hf < 2; ++hf) {
            if (hf == 1) {
                await<2>(sb, C_XN, 8);
                __builtin_amdgcn_s_setprio(3);
            }
            float v[6];
#pragma unroll
            for (int i = 0; i < 6; ++i) { v[i] = bf_lo(prm[i]); prm[i] >>= 16; }
            const float a2 = dt * v[0] * 1.4426950408889634f, bt = dt * v[1] * 0.15915494309189535f;
            const cpx c = cmul(cpx{v[4], v[5]}, cpx{dt * v[2], dt * v[3]});
            auto cexp = [&](float l) AI {
                const float e = __builtin_amdgcn_exp2f(a2 * l), ph = __builtin_amdgcn_fractf(bt * l);
                return cpx{e * __builtin_amdgcn_cosf(ph), e * __builtin_amdgcn_sinf(ph)};
            };
            auto csq = [&](cpx a) AI { return cpx{a.re * a.re - a.im * a.im, 2.f * a.re * a.im}; };
            auto put = [&](int row, cpx u) AI { lds_w32(wa + row * KG_ROW, pack2(u.re, u.im)); };
            auto get = [&](u32x4* f) AI {
                CFENCE();
#pragma unroll
                for (int k = 0; k < 4; ++k) f[k] = lds_r128(ra + k * 64);
                CFENCE();
            };
            const cpx e1 = cexp(1.f), e8 = cexp(8.f), e64 = cexp(64.f), e128 = csq(e64), e256 = csq(e128);
            u32x4 fb[4], fa[4];
            {
                const cpx c8 = {e8.re, -e8.im}, c64 = {e64.re, -e64.im};
                cpx u = c8;
                put(0, cpx{1.f, 0.f}); put(8, c64);
#pragma unroll
                for (int i = 1; i < 8; ++i) { put(i, u); put(8 + i, cmul(u, c64)); if (i < 7) u = cmul(u, c8); }
                get(fb);
            }
            cpx ev[4], ew[4];
            {
                cpx u = c;
#pragma unroll
                for (int i = 0; i < 8; ++i) {
                    const cpx u2 = cmul(u, e128);
                    put(i, u); put(8 + i, u2);
                    if ((i & 1) == 0) { ev[i >> 1] = u; ew[i >> 1] = u2; }
                    if (i < 7) u = cmul(u, e1);
                }
                get(fa);
#pragma unroll
                for (int k = 0; k < 4; ++k) k0 = mfma16(fa[k], fb[k], k0);
            }
            cpx t0, t1;
            {
#pragma unroll
                for (int i = 0; i < 4; ++i) {
                    const cpx g1 = cmul(ev[i], e256), g2 = cmul(ew[i], e256);
                    put(i, g1); put(4 + i, g2);
                    if ((i & 1) == 0) {
                        const cpx h1 = cmul(g1, e256), h2 = cmul(g2, e256), h3 = cmul(h1, e256), h4 = cmul(h2, e256);
                        put(8 + (i >> 1), h1); put(10 + (i >> 1), h2); put(12 + (i >> 1), h3); put(14 + (i >> 1), h4);
                        if (i == 0) { t0 = h3; t1 = h4; }
                    }
                }
                get(fa);
#pragma unroll
                for (int k = 0; k < 4; ++k) k1 = mfma16(fa[k], fb[k], k1);
            }
            {
#pragma unroll
                for (int i = 0; i < 5; ++i) {
                    t0 = cmul(t0, e256); put(2 * i, t0);
                    if (i < 4) { t1 = cmul(t1, e256); put(2 * i + 1, t1); }
                }
                get(fa);
#pragma unroll
                for (int k = 0; k < 4; ++k) k2 = mfma16(fb[k], fa[k], k2);
            }
        }
        MFMA_SETTLE();
        const u32x2 kc0 = pack4(k0), kc1 = pack4(k1), kc2 = pack4(k2);
        lds_w64(img + 2 * (128 * (q >> 1) + 4 * (q & 1) + 8 * r16), kc0);
        if (r16 <= 8) lds_w64(img + 2 * (512 + 16 * r16 + 4 * q), kc2);
        if (q < 2) lds_w64(img + 2 * (256 + 64 * q + 4 * r16), kc1);
        else {
            lds_w32(img + 2 * (384 + 64 * (q - 2) + 2 * r16), kc1[0]);
            lds_w32(img + 2 * (416 + 64 * (q - 2) + 2 * r16), kc1[1]);
        }
        CFENCE();
#pragma unroll
        for (int c = 0; c < 4; ++c) {
            u32 e[9];
#pragma unroll
            for (int j = 0; j < 9; ++j) {
                const int l = 2047 - 8 * (c * 64 + lane) - j;
                const int code = (int)(pack2((float)l, 0.f) & 0xffffu) - 17024;
                e[j] = *(LAS u16*)(img + 2 * (c < 3 ? code : l < 128 ? (l < 0 ? 0 : l) : code));
            }
            if (c == 3 && lane == 63) e[8] = 0;
            ke[c][0] = u32x4{e[0] | e[1] << 16, e[2] | e[3] << 16, e[4] | e[5] << 16, e[6] | e[7] << 16};
            ke[c][1] = u32x4{e[1] | e[2] << 16, e[3] | e[4] << 16, e[5] | e[6] << 16, e[7] | e[8] << 16};
            asm volatile("" : "+v"(ke[c][0]), "+v"(ke[c][1]));
        }
        sprefetch(p.w2 + (size_t)s * 663552, cx.mtile * 3 + wb, 96);
    } else {
        __builtin_amdgcn_s_setprio(3);
        u32 go0;
        asm volatile("global_load_dword %0, %1, off sc1" : "=v"(go0) : "v"(gow + wg) : "memory");
        auto touch1 = [&](int k) AI {
            const int t = k * 64 + lane, i = (t < 162 ? t : 0) * 32 + cx.mtile, row = i / 9;
            touch(p.w1 + s * 1152, (u32)(row * 9216 + (i - row * 9) * 128), sb + L_SINK);
        };
        touch1(0);
        glds16(p.b1 + s * 1152, lane * 16, sb + L_BIAS);
        glds16(p.b1 + s * 1152 + 128, lane * 16, sb + L_BIAS + 128);
#pragma unroll 1
        for (int j = 0; j < 8; ++j) {
            u32 vo[5];
            stage_offsets<true>(vo, lane, j & 3);
            const char* ga = p.w1 + s * 1152 + (j & 3) * 128 + (j >> 2) * (32 * 9216);
            const char* gb = ga + 640;
#pragma unroll
            for (int i = 0; i < 5; ++i) stage_piece<5>(ga, vo, sb + L_RINGA + (j & 3) * 10240 + (j >> 2) * 5120, 0, 0, i);
#pragma unroll
            for (int i = 0; i < 4; ++i) stage_piece<4>(gb, vo, sb + L_RINGB + (j & 3) * 8192 + (j >> 2) * 4096, 0, 0, i);
        }
#pragma unroll 1
        for (int k = 1; k < 3; ++k) touch1(k);
        asm volatile("s_waitcnt vmcnt(2)" : "+v"(go0) :: "memory");
        arrive(sb, C_W);
        const bool master = wg == 0;
        const u32 nonce = (u32)__builtin_amdgcn_readfirstlane(go0) + 1u;
        if (lane == 0) __hip_atomic_store((GAS u32*)(p.ws + WS_NONCE) + wg, nonce, RLX_AGENT);
        if (lane < 8) st128(u32x4{0, 0, 0, 0}, wsr, WS_FLAG + wg * FLAG_STRIDE + lane * 16, 0);
        if (master) st128(u32x4{0, 0, 0, 0}, wsr, WS_U + lane * 16, 0);
        {
            const u32x4 ny = {NOTYET, NOTYET, NOTYET, NOTYET};
#pragma unroll 1
            for (int i = 0; i < 8; ++i) st128(ny, wsr, WS_YB + (r16 * 512 + wg * 2) * 32 + q * 16, i * (16 * 512 * 32));
            st128(ny, wsr, WS_YC + (chC - 512) * 4096 + ((q & 1) ? 7 - qt : qt) * 512 + r16 * 32 + (q >> 1) * 16, 0);
        }
        auto echo = [&]() AI { st128(__builtin_amdgcn_raw_buffer_load_b128(wsr, WS_NONCE + lane * 16, 0, 16), wsr, WS_GO + lane * 16, 0); };
        await(sb, C_XN, 8);
        {
            const int uo = WS_UT + ((cx.mtile * 4 + (s >> 1)) * DM + (s & 1) * 288 + (q & 1) * 16 + r16) * 32 + (q >> 1) * 16;
            const u32 a = sb + s * 9 * 1024 + (((r16 >> 1) & 1) * 16 + 4 * q + (r16 >> 2)) * 16 + (r16 & 1) * 8;
#pragma unroll 1
            for (int c = 0; c < 9; ++c) st128(widen(tr_read(a + c * 1024), tr_read(a + c * 1024 + 512)), wsr, uo, c * 1024);
            arrive(sb, C_1A); arrive(sb, C_1B);
        }
        WAIT_VM(0);
#pragma unroll 1
        for (int spin = 0; spin < SPIN_CAP; ++spin) {
            if (master) echo();
            if ((u32)__builtin_amdgcn_readfirstlane(__hip_atomic_load(gow + wg, RLX_AGENT)) == nonce) break;
            __builtin_amdgcn_s_sleep(16);
        }
        if (lane == 0) __hip_atomic_store((GAS u32*)(p.ws + WS_U) + wg, 1u, RLX_AGENT);
#pragma unroll 1
        for (int spin = 0; spin < SPIN_CAP; ++spin) {
            if (master) __builtin_amdgcn_s_sleep(8); else __builtin_amdgcn_s_sleep(32);
            CFENCE();
            const u32x4 f = __builtin_amdgcn_raw_buffer_load_b128(wsr, WS_U + lane * 16, 0, 16);
            if (__builtin_amdgcn_ballot_w64((f[0] & f[1] & f[2] & f[3]) != 1u) == 0) break;
            if (master) echo();
        }
        arrive(sb, C_F);
    }
    u32 tgt = 0;
    auto sync = [&]() AI { arrive(sb, C_C); tgt += 4; await<2>(sb, C_C, tgt); };
    arrive(sb, C_C); tgt += 4; await<16>(sb, C_C, tgt);
    u32x4 uv[3];
#pragma unroll
    for (int c = 0; c < 3; ++c)
        uv[c] = __builtin_amdgcn_raw_buffer_load_b128(wsr, WS_UT + (wb * 32 + (lane >> 1)) * (DM * 32) + (lane & 1) * 16, (c < 2 ? wg * 2 + c : chC) * 32, 16);
    auto put = [&](int c, u32 cv) AI {
        if (wb == c) {
#pragma unroll
            for (int k = 0; k < 4; ++k) { lds_w128(cv + 4608 + k * 1024 + lane * 16, ke[k][0]); lds_w128(cv + 4608 + KR_ODD + k * 1024 + lane * 16, ke[k][1]); }
        }
        if (wb == 3) {
            const u32 zo = lane < 30 ? lane * 16 : lane < 34 ? 4608 + 4096 + (lane - 30) * 16 : 4608 + KR_ODD + 4096 + (lane - 34) * 16;
            if (lane < 38) lds_w128(cv + zo, u32x4{0, 0, 0, 0});
        }
        lds_w128(cv + 480 + wb * 1024 + lane * 16, uv[c]);
    };
    put(2, sb + L_C);
    sync();
    f32x4 cq[2];
    {
        const u32 cv = sb + L_C;
        if (qt == 0) conv_acc<0x81, 0x80, 2>(cv, wb * 2, lane, cq);
        else if (qt == 1) conv_acc<0x42, 0x40, 2>(cv, wb * 2, lane, cq);
        else if (qt == 2) conv_acc<0x24, 0x20, 2>(cv, wb * 2, lane, cq);
        else conv_acc<0x18, 0x18, 2>(cv, wb * 2, lane, cq);
        sync();
        const u32 xs = cv + lane * 16;
        lds_wf4(xs + wb * 2048, cq[0]); lds_wf4(xs + wb * 2048 + 1024, cq[1]);
        sync();
        if (wb == 0) {
#pragma unroll
            for (int j = 1; j < 4; ++j) { cq[0] += lds_rf4(xs + j * 2048); cq[1] += lds_rf4(xs + j * 2048 + 1024); }
            st128(published(widen(pack4(cq[0]), pack4(cq[1]))), wsr, WS_YC + (chC - 512) * 4096 + ((q & 1) ? 7 - qt : qt) * 512 + r16 * 32 + (q >> 1) * 16, 0);
        }
    }
    await<4>(sb, C_G, 4);
    __builtin_amdgcn_s_setprio(1);
    put(0, sb + L_CV1);
    put(1, sb + L_CV1 + CV_LDS);
    sync();
    {
        f32x4 ca[8];
        conv_full(sb + L_CV1 + (wb >> 1) * CV_LDS, (wb & 1) * 4, lane, ca);
        sync();
        const u32 xs = sb + L_CV1 + lane * 16;
        f32x4 o0 = ca[0], o1 = ca[1];
#pragma unroll
        for (int j = 0; j < 4; ++j) {
            if (j == wb) { o0 = ca[2 * j]; o1 = ca[2 * j + 1]; }
            else {
                const u32 o = xs + (j * 3 + (wb ^ j) - 1) * 2048;
                lds_wf4(o, ca[2 * j]); lds_wf4(o + 1024, ca[2 * j + 1]);
            }
        }
        sync();
        const u32 xr = xs + wb * 6144;
        o0 += lds_rf4(xr); o1 += lds_rf4(xr + 1024);
        const f32x4 n0 = lds_rf4(xr + 2048) + lds_rf4(xr + 4096), n1 = lds_rf4(xr + 3072) + lds_rf4(xr + 5120);
        const u32x2 a0 = pack4(o0), a1 = pack4(o1), b0 = pack4(n0), b1 = pack4(n1);
        const int yo = WS_YB + (r16 * 512 + wg * 2 + ((q ^ (wb >> 1)) & 1)) * 32 + (q >> 1) * 16;
        st128(published(widen(a0, b0)), wsr, yo, (2 * wb) * (16 * 512 * 32));
        st128(published(widen(a1, b1)), wsr, yo, (2 * wb + 1) * (16 * 512 * 32));
    }
    if (wb == 1) {
        const Side sd = load_side(p, cx, 8 + (s & 1));
        await<2>(sb, C_OK, 1);
        reduce_chunk(p, cx, sd, 8 + (s & 1), sb + L_AX + ((s & 1) * 4 + (s >> 1)) * 1024, sb + L_C + 12288);
    }
}

__global__ __launch_bounds__(768, 3) void main_kernel(MainArgs p) {
    extern __shared__ __attribute__((aligned(1024))) char dsm[];
    Ctx cx;
    const u32 sb = (u32)(size_t)(LAS char*)dsm;
    const int tid = __builtin_amdgcn_workitem_id_x();
    const int lane = tid & 63, w = __builtin_amdgcn_readfirstlane(tid >> 6);
    const int bid = __builtin_amdgcn_workgroup_id_x();
    if (w == 11) {
        const char* pc;
        asm volatile("s_getpc_b64 %0" : "=s"(pc));
        const u32 o = (u32)((lane & 15) * 32 + (bid >> 3)) * 128;
        touch(pc, o < CODE_BYTES ? o : 0, sb + L_SINK);
    }
    const int xg = bid & 7, xi = bid >> 3;
    const int s = ((xg & ((8 >> XM) - 1)) << XM) | (xi & ((1 << XM) - 1));
    const int mtile = (xg >> (3 - XM)) * (32 >> XM) + (xi >> XM);
    cx.sb = sb; cx.w = w; cx.s = s; cx.mtile = mtile; cx.wg = mtile * 8 + s;
    if (w >= 8 && w < 11) {
        const u16* const pp[6] = {p.Are, p.Aim, p.Bre, p.Bim, p.Cre, p.Cim};
        const u32 o = (u32)((w < 10 ? cx.wg * 2 + (w & 1) : 512 + (cx.wg >> 2)) * NST + lane * 2) * 2;
#pragma unroll
        for (int i = 0; i < 6; ++i) touch((const char*)pp[i], o, sb + L_C + (w & 3) * KG_IMG + i * 256);
    }
    if (w == 0 && lane < 32) lds_w32(sb + L_CNT + lane * 4, 0u);
    asm volatile("s_waitcnt lgkmcnt(0)\n\ts_barrier" ::: "memory");
    if (w >= 8) {
        ssm_body(p, cx);
        __builtin_amdgcn_endpgm();
    }
    __builtin_amdgcn_s_setprio(2);
    const int r16 = lane & 15, q = lane >> 4;
    {
        const char* xb = (const char*)p.x + ((mtile * 64 + (w >> 1) * 16) * DM + (w & 1) * 288) * 2;
#pragma unroll
        for (int i = 0; i < 9; ++i) {
            const int c = i * 64 + lane, row = (c * 1821) >> 16;
            glds16(xb, (u32)(row * 576 + c * 16), sb + w * 9216 + i * 1024);
        }
    }
    u32x4 xf[9];
    const u32 xo = sb + w * 9216, so = sb + (w < 4 ? L_BIAS - 512 : L_SINK + 256) + ((w >> 1) & 1) * 256;
    {
        const u32x4 ones = {0x3f803f80u, 0x3f803f80u, 0x3f803f80u, 0x3f803f80u};
        f32x4 s1 = {0.f, 0.f, 0.f, 0.f}, s2 = {0.f, 0.f, 0.f, 0.f};
        WAIT_VM(0);
#pragma unroll
        for (int i = 0; i < 9; ++i) xf[i] = lds_r128(xo + r16 * 576 + i * 64 + q * 16);
#pragma unroll
        for (int i = 0; i < 9; ++i) { s1 = mfma16(ones, xf[i], s1); s2 = mfma16(xf[i], xf[i], s2); }
        MFMA_SETTLE();
        const int r = r16 & 3;
        const f32x2 st = {s1[0], r == 0 ? s2[0] : r == 1 ? s2[1] : r == 2 ? s2[2] : s2[3]};
        if (q == (r16 >> 2)) *(LAS f32x2*)(so + r16 * 16 + (w & 1) * 8) = st;
    }
    arrive(sb, C_S + (w >> 1));
    await(sb, C_S + (w >> 1), 2);
    {
        const f32x4 st = lds_rf4(so + r16 * 16);
        const float m = (st[0] + st[2]) * (1.f / DM);
        const float var = rbf(__builtin_fmaxf((st[1] + st[3]) * (1.f / DM) - m * m, 0.f));
        const float rs = __builtin_amdgcn_rcpf(rbf(__builtin_amdgcn_sqrtf(rbf(var + 1e-5f)))), sh = -rbf(m) * rs;
#pragma unroll
        for (int i = 0; i < 9; ++i) {
            u32x4 o;
#pragma unroll
            for (int j = 0; j < 4; ++j) o[j] = pack2(__builtin_fmaf(bf_lo(xf[i][j]), rs, sh), __builtin_fmaf(bf_hi(xf[i][j]), rs, sh));
            lds_w128(xo + i * 1024 + lane * 16, o);
        }
    }
    arrive(sb, C_XN);
    if (w < 4) { mlp_body<5>(p, cx); __builtin_amdgcn_endpgm(); }
    mlp_body<4>(p, cx);
}
#if defined(__HIP_DEVICE_COMPILE__)
#pragma clang attribute pop
#endif

extern "C" void kernel_launch(void* const* d_in, const int* in_sizes, int n_in, void* d_out, int out_size,
                              void* d_ws, size_t ws_size, hipStream_t stream) {
    (void)hipFuncSetAttribute((const void*)main_kernel, hipFuncAttributeMaxDynamicSharedMemorySize, MAIN_LDS);
    MainArgs a{(const u16*)d_in[0], (const u16*)d_in[1], (const u16*)d_in[2], (const u16*)d_in[3], (const u16*)d_in[4],
               (const u16*)d_in[5], (const u16*)d_in[6], (const u32*)d_in[7], (const u32*)d_in[8], (const char*)d_in[10],
               (const char*)d_in[11], (const char*)d_in[12], (const u16*)d_in[13], (char*)d_ws, (u16*)d_out};
    main_kernel<<<256, 768, MAIN_LDS, stream>>>(a);
}
```
